```python
import torch

def _softmax(x, axis=-1):
    return torch.softmax(x, dim=axis)
def _gelu(x):
    return torch.nn.functional.gelu(x, approximate="tanh")
def _silu(x):
    return torch.nn.functional.silu(x)
def _sigmoid(x):
    return torch.sigmoid(x)
def _layernorm(x, axis=-1, eps=1e-5):
    m = x.mean(dim=axis, keepdim=True)
    v = x.var(dim=axis, keepdim=True, unbiased=False)
    return (x - m) / torch.sqrt(v + eps)
def _scatter_add(tgt, idx, src, axis=0):
    return tgt.clone().index_add_(axis, idx, src)
def _segment_sum(src, seg, n):
    out = torch.zeros((n,) + src.shape[1:], dtype=src.dtype)
    return out.index_add_(0, seg, src)
def _pad(x, pw):
    flat = tuple(v for pair in reversed(pw) for v in pair)
    return torch.nn.functional.pad(x, flat)

kernel_name = 'standalone__linear_attn_block__prompt'

def setup_inputs():

    _w = (lambda a: (lambda t: t.to(torch.bfloat16) if t.is_floating_point() else t)(torch.as_tensor(a)))
    B, S, D = 1, 2048, 640
    return {
        "x": _w((lambda *s: torch.randn(*s, dtype=torch.bfloat16) * 0.05)(B, S, D)),
        "wqkv": _w((lambda *s: torch.randn(*s, dtype=torch.bfloat16) * 0.4)(D, 3 * D)),
        "wo": _w((lambda *s: torch.randn(*s, dtype=torch.bfloat16) * 0.05)(D, D)),
    }

def reference(x, wqkv, wo):
    B, S, D = x.shape
    H = 32; Dh = D // H
    qkv = (lambda x, *d: x.permute(*d))((x @ wqkv).reshape(B, S, 3, H, Dh), 2, 0, 3, 1, 4)
    q, k, v = qkv[0], qkv[1], qkv[2]
    phi = lambda t: torch.where(t > 0, t + 1, torch.exp(t)) + 1e-6
    pq, pk = phi(q), phi(k)
    kv = torch.einsum("bhsd,bhse->bhde", pk, v)
    z = torch.einsum("bhsd->bhd", pk)
    num = torch.einsum("bhqd,bhde->bhqe", pq, kv)
    den = torch.einsum("bhqd,bhd->bhq", pq, z)[..., None] + 1e-6
    o = num / den
    return (lambda x, *d: x.permute(*d))(o, 0, 2, 1, 3).reshape(B, S, D) @ wo
```

```cpp
#include <hip/hip_runtime.h>
#include <hip/hip_bf16.h>
#include <stdint.h>


typedef unsigned short u16;
typedef __attribute__((ext_vector_type(8))) short s16x8;
typedef __attribute__((ext_vector_type(4))) short s16x4;
typedef __attribute__((ext_vector_type(4))) float f32x4;
typedef __attribute__((ext_vector_type(2))) unsigned int u32x2;
typedef __attribute__((ext_vector_type(4))) unsigned int u32x4;

constexpr int S_ = 2048, D_ = 640, H_ = 32, DH_ = 20, N3_ = 1920;
constexpr int NRT = 16;
constexpr int KVN = 420;

#define LDS_AS __attribute__((address_space(3)))

__device__ __forceinline__ float bf2f(u16 u) { return __uint_as_float(((unsigned)u) << 16); }
__device__ __forceinline__ u16 f2bf(float f) { __bf16 b = (__bf16)f; return __builtin_bit_cast(u16, b); }
__device__ __forceinline__ float rbf(float f) { return bf2f(f2bf(f)); }
__device__ __forceinline__ unsigned pack2(float a, float b) { return (unsigned)f2bf(a) | ((unsigned)f2bf(b) << 16); }
__device__ __forceinline__ float phi(float acc) {
    float t = rbf(acc);
    float p = t > 0.f ? rbf(t + 1.0f) : rbf(__expf(t));
    return rbf(p + 1e-6f);
}

#define WAIT_V(n) asm volatile("s_waitcnt vmcnt(" #n ")" ::: "memory")
#define WAIT_L(n) asm volatile("s_waitcnt lgkmcnt(" #n ")" ::: "memory")
#define BAR() __builtin_amdgcn_s_barrier()

__device__ __forceinline__ s16x4 tr_read(const char* lds_ptr) {
    return __builtin_amdgcn_ds_read_tr16_b64_v4i16((LDS_AS s16x4*)lds_ptr);
}
__device__ __forceinline__ s16x8 cat8(s16x4 a, s16x4 b) {
    s16x8 r; r[0]=a[0]; r[1]=a[1]; r[2]=a[2]; r[3]=a[3]; r[4]=b[0]; r[5]=b[1]; r[6]=b[2]; r[7]=b[3]; return r;
}

constexpr int K1_A_BYTES = 128 * 64 * 2;
constexpr int K1_B_BYTES = 64 * 160 * 2;
constexpr int K1_STAGE = K1_A_BYTES + K1_B_BYTES;
constexpr int K1_NST = 4;

template <bool IS_Q>
__device__ __forceinline__ void k1_body(const u16* __restrict__ x, const u16* __restrict__ w,
                                        u16* __restrict__ pq, float* __restrict__ part,
                                        char* smem, int rt, int cg) {
    const int tid = threadIdx.x, lane = tid & 63, wv = tid >> 6;
    const int g = lane >> 4, r16 = lane & 15;
    const int row0 = rt * 128;

    int a_src[4], b_src[5];
#pragma unroll
    for (int i = 0; i < 4; ++i) {
        int ins = wv * 4 + i;
        int row = ins * 8 + (lane >> 3), slot = lane & 7;
        a_src[i] = (row0 + row) * D_ + ((slot ^ (row & 7)) * 8);
    }
#pragma unroll
    for (int i = 0; i < 5; ++i) {
        int c = (wv * 5 + i) * 64 + lane;
        int krow = c / 20, cc = c % 20;
        int sc = cc ^ (((krow >> 3) & 1) << 1);
        int col = sc * 8;
        int gcol;
        if (IS_Q) gcol = cg * 160 + col;
        else gcol = (col < 80) ? (640 + (cg - 4) * 80 + col) : (1280 + (cg - 4) * 80 + (col - 80));
        b_src[i] = krow * N3_ + gcol;
    }

    auto stage = [&](int st, int kt) {
        char* a_dst = smem + st * K1_STAGE;
        char* b_dst = a_dst + K1_A_BYTES;
#pragma unroll
        for (int i = 0; i < 4; ++i)
            __builtin_amdgcn_global_load_lds((const unsigned*)(x + a_src[i] + kt * 64),
                                             (unsigned*)(a_dst + (wv * 4 + i) * 1024 + lane * 16), 16, 0, 0);
#pragma unroll
        for (int i = 0; i < 5; ++i)
            __builtin_amdgcn_global_load_lds((const unsigned*)(w + b_src[i] + kt * 64 * N3_),
                                             (unsigned*)(b_dst + (wv * 5 + i) * 1024 + lane * 16), 16, 0, 0);
    };

    f32x4 acc[2][10];
#pragma unroll
    for (int m = 0; m < 2; ++m)
#pragma unroll
        for (int n = 0; n < 10; ++n) acc[m][n] = (f32x4){0.f, 0.f, 0.f, 0.f};

    stage(0, 0); stage(1, 1); stage(2, 2);

    int a_off[2][2];
#pragma unroll
    for (int m = 0; m < 2; ++m)
#pragma unroll
        for (int ks = 0; ks < 2; ++ks) {
            int row = wv * 32 + m * 16 + r16;
            a_off[m][ks] = row * 128 + (((4 * ks + g) ^ (row & 7)) * 16);
        }
    const int q4 = (lane & 15) >> 2, p4 = lane & 3;
    const int b_off0 = (8 * g + q4) * 320 + p4 * 8;

    for (int kt = 0; kt < 10; ++kt) {
        if (kt < 8) WAIT_V(18); else if (kt == 8) WAIT_V(9); else WAIT_V(0);
        BAR();
        if (kt + 3 < 10) stage((kt + 3) & 3, kt + 3);
        const char* a_base = smem + (kt & 3) * K1_STAGE;
        const char* b_base = a_base + K1_A_BYTES;
#pragma unroll
        for (int ks = 0; ks < 2; ++ks) {
            s16x8 af[2];
#pragma unroll
            for (int m = 0; m < 2; ++m) af[m] = *(const s16x8*)(a_base + a_off[m][ks]);
#pragma unroll
            for (int n = 0; n < 10; ++n) {
                const char* p = b_base + b_off0 + (32 * ks) * 320 + ((n ^ (g & 1)) * 32);
                s16x8 bfr = cat8(tr_read(p), tr_read(p + 4 * 320));
#pragma unroll
                for (int m = 0; m < 2; ++m) {
                    if (IS_Q) acc[m][n] = __builtin_amdgcn_mfma_f32_16x16x32_bf16(bfr, af[m], acc[m][n], 0, 0, 0);
                    else      acc[m][n] = __builtin_amdgcn_mfma_f32_16x16x32_bf16(af[m], bfr, acc[m][n], 0, 0, 0);
                }
            }
        }
    }

    if (IS_Q) {
#pragma unroll
        for (int m = 0; m < 2; ++m) {
            int s = row0 + wv * 32 + m * 16 + r16;
#pragma unroll
            for (int n = 0; n < 10; ++n) {
                u32x2 v;
                v[0] = pack2(phi(acc[m][n][0]), phi(acc[m][n][1]));
                v[1] = pack2(phi(acc[m][n][2]), phi(acc[m][n][3]));
                *(u32x2*)(pq + (size_t)s * D_ + cg * 160 + n * 16 + 4 * g) = v;
            }
        }
    } else {
        s16x8 fr[10];
#pragma unroll
        for (int n = 0; n < 10; ++n) {
#pragma unroll
            for (int j = 0; j < 4; ++j) {
                float a0 = acc[0][n][j], a1 = acc[1][n][j];
                if (n < 5) { a0 = phi(a0); a1 = phi(a1); }
                fr[n][j] = (short)f2bf(a0);
                fr[n][4 + j] = (short)f2bf(a1);
            }
        }
        s16x8 ones;
#pragma unroll
        for (int j = 0; j < 8; ++j) ones[j] = (short)0x3F80;
        BAR();
        float* red = (float*)smem + wv * (4 * KVN);
        const f32x4 zero4 = (f32x4){0.f, 0.f, 0.f, 0.f};
#pragma unroll
        for (int kt2 = 0; kt2 < 5; ++kt2) {
#pragma unroll
            for (int dv = -1; dv <= 1; ++dv) {
                int vt = kt2 + dv;
                if (vt < 0 || vt > 4) continue;
                f32x4 dd = __builtin_amdgcn_mfma_f32_16x16x32_bf16(fr[kt2], fr[5 + vt], zero4, 0, 0, 0);
                int e = vt * 16 + r16;
                int he = e / 20;
#pragma unroll
                for (int r = 0; r < 4; ++r) {
                    int d = kt2 * 16 + 4 * g + r;
                    if (d / 20 == he) red[he * KVN + (d - he * 20) * 20 + (e - he * 20)] = dd[r];
                }
            }
            f32x4 zz = __builtin_amdgcn_mfma_f32_16x16x32_bf16(fr[kt2], ones, zero4, 0, 0, 0);
            if (r16 == 0) {
#pragma unroll
                for (int r = 0; r < 4; ++r) {
                    int d = kt2 * 16 + 4 * g + r;
                    int hd = d / 20;
                    red[hd * KVN + 400 + (d - hd * 20)] = zz[r];
                }
            }
        }
        __syncthreads();
        const float* rb = (const float*)smem;
        float* dst = part + ((size_t)rt * H_ + (cg - 4) * 4) * KVN;
        for (int i = tid; i < 4 * KVN; i += 256)
            dst[i] = (rb[i] + rb[4 * KVN + i]) + (rb[8 * KVN + i] + rb[12 * KVN + i]);
    }
}

__global__ __launch_bounds__(256) void k1_kernel(const u16* __restrict__ x, const u16* __restrict__ w,
                                                 u16* __restrict__ pq, float* __restrict__ part) {
    __shared__ __attribute__((aligned(16))) char smem[K1_NST * K1_STAGE];
    int b = blockIdx.x;
    int xcd = b & 7, idx = b >> 3;
    int rt = (xcd >> 1) * 4 + (idx & 3);
    int cg = (xcd & 1) * 6 + (idx >> 2);
    if (cg < 4) k1_body<true>(x, w, pq, part, smem, rt, cg);
    else        k1_body<false>(x, w, pq, part, smem, rt, cg);
}

__global__ __launch_bounds__(512) void kr_kernel(const float* __restrict__ part, u16* __restrict__ kvfrag) {
    __shared__ float red[KVN];
    int h = blockIdx.x, tid = threadIdx.x;
    if (tid < KVN) {
        float v[NRT];
#pragma unroll
        for (int p = 0; p < NRT; ++p) v[p] = part[((size_t)p * H_ + h) * KVN + tid];
        float s = 0.f;
#pragma unroll
        for (int p = 0; p < NRT; ++p) s += v[p];
        red[tid] = rbf(s);
    }
    __syncthreads();
    if (tid < 128) {
        int t = tid >> 6, lane = tid & 63, g = lane >> 4;
        int row = 16 * t + (lane & 15);
        s16x8 o;
#pragma unroll
        for (int j = 0; j < 8; ++j) {
            int k = 8 * g + j;
            float v = 0.f;
            if (k < 20) {
                if (row < 20) v = red[k * 20 + row];
                else if (row == 20) v = red[400 + k];
            }
            o[j] = (short)f2bf(v);
        }
        *(s16x8*)(kvfrag + ((size_t)(h * 2 + t) * 64 + lane) * 8) = o;
    }
}

constexpr int OST = 648;
constexpr int K2_O_BYTES = 64 * OST * 2;
constexpr int K2_CH_BYTES = 128 * 80 * 2;

__global__ __launch_bounds__(256) void k2_kernel(const u16* __restrict__ pq, const u16* __restrict__ kvfrag,
                                                 const u16* __restrict__ wo, u16* __restrict__ out) {
    __shared__ __attribute__((aligned(16))) char smem[K2_O_BYTES + 3 * K2_CH_BYTES];
    const int tid = threadIdx.x, lane = tid & 63, wv = tid >> 6;
    const int g = lane >> 4, r16 = lane & 15;
    int b = blockIdx.x;
    int xcd = b & 7, idx = b >> 3;
    const int rt = xcd * 4 + (idx & 3), ct = idx >> 2;
    const int row0 = rt * 64, col0 = ct * 80;
    u16* oimg = (u16*)smem;
    char* wbuf = smem + K2_O_BYTES;

    int w_src[5];
#pragma unroll
    for (int i = 0; i < 5; ++i) {
        int c = (wv * 5 + i) * 64 + lane;
        int krow = c / 10, cc = c % 10;
        w_src[i] = krow * D_ + col0 + cc * 8;
    }
    auto stage = [&](int slot, int ch) {
        char* dst = wbuf + slot * K2_CH_BYTES;
#pragma unroll
        for (int i = 0; i < 5; ++i)
            __builtin_amdgcn_global_load_lds((const unsigned*)(wo + w_src[i] + ch * 128 * D_),
                                             (unsigned*)(dst + (wv * 5 + i) * 1024 + lane * 16), 16, 0, 0);
    };
    stage(0, 0); stage(1, 1); stage(2, 2);

    const f32x4 zero4 = (f32x4){0.f, 0.f, 0.f, 0.f};
#pragma unroll 2
    for (int hh = 0; hh < 8; ++hh) {
        int h = wv * 8 + hh;
        s16x8 a0 = *(const s16x8*)(kvfrag + ((size_t)(h * 2 + 0) * 64 + lane) * 8);
        s16x8 a1 = *(const s16x8*)(kvfrag + ((size_t)(h * 2 + 1) * 64 + lane) * 8);
        s16x8 bq[4];
#pragma unroll
        for (int st = 0; st < 4; ++st) {
            u32x4 v = (u32x4){0u, 0u, 0u, 0u};
            if (g < 3) {
                const u16* src = pq + (size_t)(row0 + st * 16 + r16) * D_ + h * 20 + 8 * g;
                u32x2 lo = *(const u32x2*)src;
                v[0] = lo[0]; v[1] = lo[1];
                if (g < 2) { u32x2 hi = *(const u32x2*)(src + 4); v[2] = hi[0]; v[3] = hi[1]; }
            }
            bq[st] = __builtin_bit_cast(s16x8, v);
        }
#pragma unroll
        for (int st = 0; st < 4; ++st) {
            f32x4 d1 = __builtin_amdgcn_mfma_f32_16x16x32_bf16(a0, bq[st], zero4, 0, 0, 0);
            f32x4 d2 = __builtin_amdgcn_mfma_f32_16x16x32_bf16(a1, bq[st], zero4, 0, 0, 0);
            float den = __shfl(d2[0], 16 + r16);
            den = rbf(rbf(den) + 1e-6f);
            float inv = 1.0f / den;
            u16* orow = oimg + (st * 16 + r16) * OST + h * 20;
            u32x2 v;
            v[0] = pack2(rbf(d1[0]) * inv, rbf(d1[1]) * inv);
            v[1] = pack2(rbf(d1[2]) * inv, rbf(d1[3]) * inv);
            *(u32x2*)(orow + 4 * g) = v;
            if (g == 0) {
                u32x2 v2;
                v2[0] = pack2(rbf(d2[0]) * inv, rbf(d2[1]) * inv);
                v2[1] = pack2(rbf(d2[2]) * inv, rbf(d2[3]) * inv);
                *(u32x2*)(orow + 16) = v2;
            }
        }
    }

    f32x4 acc[5];
#pragma unroll
    for (int n = 0; n < 5; ++n) acc[n] = zero4;
    const int q4 = (lane & 15) >> 2, p4 = lane & 3;
    const int w_off0 = (8 * g + q4) * 160 + p4 * 8;
    const u16* orow = oimg + (wv * 16 + r16) * OST + 8 * g;

    for (int ch = 0; ch < 5; ++ch) {
        if (ch == 0) WAIT_V(10); else if (ch < 4) WAIT_V(5); else WAIT_V(0);
        WAIT_L(0);
        BAR();
        if (ch >= 1 && ch + 2 < 5) stage((ch + 2) % 3, ch + 2);
        const char* wb = wbuf + (ch % 3) * K2_CH_BYTES;
#pragma unroll
        for (int ks = 0; ks < 4; ++ks) {
            s16x8 of = *(const s16x8*)(orow + ch * 128 + ks * 32);
#pragma unroll
            for (int n = 0; n < 5; ++n) {
                const char* p = wb + w_off0 + (32 * ks) * 160 + n * 32;
                s16x8 wf = cat8(tr_read(p), tr_read(p + 4 * 160));
                acc[n] = __builtin_amdgcn_mfma_f32_16x16x32_bf16(wf, of, acc[n], 0, 0, 0);
            }
        }
    }
    int s = row0 + wv * 16 + r16;
#pragma unroll
    for (int n = 0; n < 5; ++n) {
        u32x2 v;
        v[0] = pack2(acc[n][0], acc[n][1]);
        v[1] = pack2(acc[n][2], acc[n][3]);
        *(u32x2*)(out + (size_t)s * D_ + col0 + n * 16 + 4 * g) = v;
    }
}

extern "C" void kernel_launch(void* const* d_in, const int* in_sizes, int n_in,
                              void* d_out, int out_size, void* d_ws, size_t ws_size,
                              hipStream_t stream) {
    const u16* x = (const u16*)d_in[0];
    const u16* wqkv = (const u16*)d_in[1];
    const u16* wo = (const u16*)d_in[2];
    u16* out = (u16*)d_out;
    char* ws = (char*)d_ws;
    u16* pq = (u16*)ws;
    float* part = (float*)(ws + 4u * 1024 * 1024);
    u16* kvfrag = (u16*)(ws + 6u * 1024 * 1024);
    k1_kernel<<<192, 256, 0, stream>>>(x, wqkv, pq, part);
    kr_kernel<<<32, 512, 0, stream>>>(part, kvfrag);
    k2_kernel<<<256, 256, 0, stream>>>(pq, kvfrag, wo, out);
}
```

```cpp
#include <hip/hip_runtime.h>
#include <hip/hip_bf16.h>
#include <stdint.h>

typedef unsigned short u16;
typedef __attribute__((ext_vector_type(8))) short s16x8;
typedef __attribute__((ext_vector_type(4))) short s16x4;
typedef __attribute__((ext_vector_type(4))) float f32x4;
typedef __attribute__((ext_vector_type(2))) unsigned int u32x2;
typedef __attribute__((ext_vector_type(4))) unsigned int u32x4;

constexpr int S_ = 2048, D_ = 640, H_ = 32, DH_ = 20, N3_ = 1920;
constexpr int NRT = 16;
constexpr int PSZ = 13 * 256 + 80;

#define LDS_AS __attribute__((address_space(3)))

#define STAMP(k,i)

__device__ __forceinline__ float bf2f(u16 u) { return __uint_as_float(((unsigned)u) << 16); }
__device__ __forceinline__ u16 f2bf(float f) { __bf16 b = (__bf16)f; return __builtin_bit_cast(u16, b); }
__device__ __forceinline__ float rbf(float f) { return bf2f(f2bf(f)); }
__device__ __forceinline__ unsigned pack2(float a, float b) { return (unsigned)f2bf(a) | ((unsigned)f2bf(b) << 16); }
__device__ __forceinline__ float phi(float acc) {
    float t = rbf(acc);
    float p = t > 0.f ? t + 1.0f : __expf(t);
    return rbf(rbf(p) + 1e-6f);
}

#define WAIT_V(n) asm volatile("s_waitcnt vmcnt(" #n ")" ::: "memory")
#define WAIT_L(n) asm volatile("s_waitcnt lgkmcnt(" #n ")" ::: "memory")
#define BAR() __builtin_amdgcn_s_barrier()

__device__ __forceinline__ s16x4 tr_read(const char* lds_ptr) {
    return __builtin_amdgcn_ds_read_tr16_b64_v4i16((LDS_AS s16x4*)lds_ptr);
}
__device__ __forceinline__ s16x8 cat8(s16x4 a, s16x4 b) {
    s16x8 r; r[0]=a[0]; r[1]=a[1]; r[2]=a[2]; r[3]=a[3]; r[4]=b[0]; r[5]=b[1]; r[6]=b[2]; r[7]=b[3]; return r;
}

constexpr int K1_A_BYTES = 128 * 64 * 2;
constexpr int K1_B_BYTES = 64 * 160 * 2;
constexpr int K1_STAGE = K1_A_BYTES + K1_B_BYTES;
constexpr int K1_NST = 4;

template <bool IS_Q>
__device__ __forceinline__ void k1_body(const u16* __restrict__ x, const u16* __restrict__ w,
                                        u16* __restrict__ pq, float* __restrict__ part,
                                        char* smem, int rt, int cg) {
    const int tid = threadIdx.x, lane = tid & 63;
    const int wv = __builtin_amdgcn_readfirstlane(tid >> 6);
    const int wr = wv & 3, wc = wv >> 2;
    const int g = lane >> 4, r16 = lane & 15;
    const int row0 = rt * 128;

    int a_src[2], b_src[3];
#pragma unroll
    for (int i = 0; i < 2; ++i) {
        int ins = wv * 2 + i;
        int row = ins * 8 + (lane >> 3), slot = lane & 7;
        a_src[i] = (row0 + row) * D_ + ((slot ^ (row & 7)) * 8);
    }
    const int b_ins0 = wv < 4 ? wv * 3 : 12 + (wv - 4) * 2;
#pragma unroll
    for (int i = 0; i < 3; ++i) {
        int c = (b_ins0 + i) * 64 + lane;
        if (c >= 1280) c = 0;
        int krow = c / 20, cc = c % 20;
        int sc = cc ^ (((krow >> 3) & 1) << 1);
        int col = sc * 8;
        int gcol;
        if (IS_Q) gcol = cg * 160 + col;
        else gcol = (col < 80) ? (640 + (cg - 4) * 80 + col) : (1280 + (cg - 4) * 80 + (col - 80));
        b_src[i] = krow * N3_ + gcol;
    }

    auto stage = [&](int st, int kt) {
        char* a_dst = smem + st * K1_STAGE;
        char* b_dst = a_dst + K1_A_BYTES;
#pragma unroll
        for (int i = 0; i < 2; ++i)
            __builtin_amdgcn_global_load_lds((const unsigned*)(x + a_src[i] + kt * 64),
                                             (unsigned*)(a_dst + (wv * 2 + i) * 1024 + lane * 16), 16, 0, 0);
#pragma unroll
        for (int i = 0; i < 2; ++i)
            __builtin_amdgcn_global_load_lds((const unsigned*)(w + b_src[i] + kt * 64 * N3_),
                                             (unsigned*)(b_dst + (b_ins0 + i) * 1024 + lane * 16), 16, 0, 0);
        if (wv < 4)
            __builtin_amdgcn_global_load_lds((const unsigned*)(w + b_src[2] + kt * 64 * N3_),
                                             (unsigned*)(b_dst + (b_ins0 + 2) * 1024 + lane * 16), 16, 0, 0);
    };

    f32x4 acc[2][5];
#pragma unroll
    for (int m = 0; m < 2; ++m)
#pragma unroll
        for (int n = 0; n < 5; ++n) acc[m][n] = (f32x4){0.f, 0.f, 0.f, 0.f};

    STAMP(0,0);
    stage(0, 0); stage(1, 1); stage(2, 2);

    int a_off[2][2];
#pragma unroll
    for (int m = 0; m < 2; ++m)
#pragma unroll
        for (int ks = 0; ks < 2; ++ks) {
            int row = wr * 32 + m * 16 + r16;
            a_off[m][ks] = row * 128 + (((4 * ks + g) ^ (row & 7)) * 16);
        }
    const int q4 = (lane & 15) >> 2, p4 = lane & 3;
    int b_off[5];
#pragma unroll
    for (int n = 0; n < 5; ++n) b_off[n] = (8 * g + q4) * 320 + p4 * 8 + (((wc * 5 + n) ^ (g & 1)) * 32);

#pragma unroll
    for (int kt = 0; kt < 10; ++kt) {
        if (wv < 4) { if (kt < 8) WAIT_V(10); else if (kt == 8) WAIT_V(5); else WAIT_V(0); }
        else        { if (kt < 8) WAIT_V(8);  else if (kt == 8) WAIT_V(4); else WAIT_V(0); }
        BAR();
        STAMP(0,1+kt);
        if (kt + 3 < 10) stage((kt + 3) & 3, kt + 3);
        const char* a_base = smem + (kt & 3) * K1_STAGE;
        const char* b_base = a_base + K1_A_BYTES;
#pragma unroll
        for (int ks = 0; ks < 2; ++ks) {
            s16x8 af[2];
#pragma unroll
            for (int m = 0; m < 2; ++m) af[m] = *(const s16x8*)(a_base + a_off[m][ks]);
#pragma unroll
            for (int n = 0; n < 5; ++n) {
                const char* p = b_base + b_off[n] + (32 * ks) * 320;
                s16x8 bfr = cat8(tr_read(p), tr_read(p + 4 * 320));
#pragma unroll
                for (int m = 0; m < 2; ++m) {
                    if (IS_Q) acc[m][n] = __builtin_amdgcn_mfma_f32_16x16x32_bf16(bfr, af[m], acc[m][n], 0, 0, 0);
                    else      acc[m][n] = __builtin_amdgcn_mfma_f32_16x16x32_bf16(af[m], bfr, acc[m][n], 0, 0, 0);
                }
            }
        }
    }
    STAMP(0,11);

    if (IS_Q) {
#pragma unroll
        for (int m = 0; m < 2; ++m) {
            int s = row0 + wr * 32 + m * 16 + r16;
#pragma unroll
            for (int n = 0; n < 5; ++n) {
                u32x2 v;
                v[0] = pack2(phi(acc[m][n][0]), phi(acc[m][n][1]));
                v[1] = pack2(phi(acc[m][n][2]), phi(acc[m][n][3]));
                *(u32x2*)(pq + (size_t)s * D_ + cg * 160 + wc * 80 + n * 16 + 4 * g) = v;
            }
        }
    } else {
        s16x8 fr[5];
#pragma unroll
        for (int n = 0; n < 5; ++n) {
#pragma unroll
            for (int j = 0; j < 4; ++j) {
                float a0 = acc[0][n][j], a1 = acc[1][n][j];
                if (wc == 0) { a0 = phi(a0); a1 = phi(a1); }
                fr[n][j] = (short)f2bf(a0);
                fr[n][4 + j] = (short)f2bf(a1);
            }
        }
        BAR();
        char* xch = smem + 64 * 1024;
        if (wc == 1) {
#pragma unroll
            for (int n = 0; n < 5; ++n) *(s16x8*)(xch + ((wr * 5 + n) * 64 + lane) * 16) = fr[n];
        }
        __syncthreads();
        if (wc == 0) {
            s16x8 vf[5];
#pragma unroll
            for (int n = 0; n < 5; ++n) vf[n] = *(const s16x8*)(xch + ((wr * 5 + n) * 64 + lane) * 16);
            s16x8 ones;
#pragma unroll
            for (int j = 0; j < 8; ++j) ones[j] = (short)0x3F80;
            float* red = (float*)smem + wr * PSZ;
            const f32x4 zero4 = (f32x4){0.f, 0.f, 0.f, 0.f};
            int pi = 0;
#pragma unroll
            for (int kt2 = 0; kt2 < 5; ++kt2) {
#pragma unroll
                for (int dv = -1; dv <= 1; ++dv) {
                    int vt = kt2 + dv;
                    if (vt < 0 || vt > 4) continue;
                    f32x4 dd = __builtin_amdgcn_mfma_f32_16x16x32_bf16(fr[kt2], vf[vt], zero4, 0, 0, 0);
#pragma unroll
                    for (int r = 0; r < 4; ++r) red[pi * 256 + (4 * g + r) * 16 + r16] = dd[r];
                    ++pi;
                }
                f32x4 zz = __builtin_amdgcn_mfma_f32_16x16x32_bf16(fr[kt2], ones, zero4, 0, 0, 0);
                if (r16 == 0) *(f32x4*)(red + 13 * 256 + kt2 * 16 + 4 * g) = zz;
            }
        }
        __syncthreads();
        const float* rb = (const float*)smem;
        float* dst = part + ((size_t)rt * 8 + (cg - 4)) * PSZ;
        for (int i = tid; i < PSZ; i += 512)
            dst[i] = (rb[i] + rb[PSZ + i]) + (rb[2 * PSZ + i] + rb[3 * PSZ + i]);
    }
    STAMP(0,12);
}

__global__ __launch_bounds__(512) void k1_kernel(const u16* __restrict__ x, const u16* __restrict__ w,
                                                 u16* __restrict__ pq, float* __restrict__ part) {
    __shared__ __attribute__((aligned(16))) char smem[K1_NST * K1_STAGE];
    int b = blockIdx.x;
    int xcd = b & 7, idx = b >> 3;
    int rt = (xcd >> 1) * 4 + (idx & 3);
    int cg = (xcd & 1) * 6 + (idx >> 2);
    if (cg < 4) k1_body<true>(x, w, pq, part, smem, rt, cg);
    else        k1_body<false>(x, w, pq, part, smem, rt, cg);
}

__global__ __launch_bounds__(512) void kr_kernel(const float* __restrict__ part, u16* __restrict__ kvfrag) {
    __shared__ float red[420];
    int h = blockIdx.x, tid = threadIdx.x;
    STAMP(1,0);
    if (tid < 420) {
        int grp = h >> 2, hh = h & 3;
        int idx;
        if (tid < 400) {
            int d = hh * 20 + tid / 20, e = hh * 20 + tid % 20;
            int kt = d >> 4, vt = e >> 4;
            int pi = kt == 0 ? vt : (3 * kt - 1 + (vt - kt + 1));
            idx = pi * 256 + (d & 15) * 16 + (e & 15);
        } else {
            idx = 13 * 256 + hh * 20 + (tid - 400);
        }
        float v[NRT];
#pragma unroll
        for (int p = 0; p < NRT; ++p) v[p] = part[((size_t)p * 8 + grp) * PSZ + idx];
        float s = 0.f;
#pragma unroll
        for (int p = 0; p < NRT; ++p) s += v[p];
        red[tid] = rbf(s);
    }
    __syncthreads();
    STAMP(1,1);
    if (tid < 128) {
        int t = tid >> 6, lane = tid & 63, g = lane >> 4;
        int row = 16 * t + (lane & 15);
        s16x8 o;
#pragma unroll
        for (int j = 0; j < 8; ++j) {
            int k = 8 * g + j;
            float v = 0.f;
            if (k < 20) {
                if (row < 20) v = red[k * 20 + row];
                else if (row == 20) v = red[400 + k];
            }
            o[j] = (short)f2bf(v);
        }
        *(s16x8*)(kvfrag + ((size_t)(h * 2 + t) * 64 + lane) * 8) = o;
    }
    STAMP(1,2);
}

constexpr int K2_O_BYTES = 64 * 640 * 2;
constexpr int K2_CH_BYTES = 128 * 80 * 2;

__device__ __forceinline__ int oimg_off(int row, int col) {
    return (col >> 6) * 8192 + row * 128 + ((((col >> 3) & 7) ^ (row & 7)) << 4) + ((col & 4) << 1);
}

__global__ __launch_bounds__(512) void k2_kernel(const u16* __restrict__ pq, const u16* __restrict__ kvfrag,
                                                 const u16* __restrict__ wo, u16* __restrict__ out) {
    __shared__ __attribute__((aligned(16))) char smem[K2_O_BYTES + 3 * K2_CH_BYTES];
    const int tid = threadIdx.x, lane = tid & 63;
    const int wv = __builtin_amdgcn_readfirstlane(tid >> 6);
    const int g = lane >> 4, r16 = lane & 15;
    int b = blockIdx.x;
    int xcd = b & 7, idx = b >> 3;
    const int rt = xcd * 4 + (idx & 3), ct = idx >> 2;
    const int row0 = rt * 64, col0 = ct * 80;
    char* oimg = smem;
    char* wbuf = smem + K2_O_BYTES;

    STAMP(2,0);
#pragma unroll
    for (int j = 0; j < 10; ++j) {
        int i = wv * 10 + j;
        int blk = i >> 3, row = (i & 7) * 8 + (lane >> 3), slot = lane & 7;
        const u16* src = pq + (size_t)(row0 + row) * D_ + blk * 64 + ((slot ^ (row & 7)) * 8);
        __builtin_amdgcn_global_load_lds((const unsigned*)src, (unsigned*)(oimg + i * 1024 + lane * 16), 16, 0, 0);
    }
    s16x8 a0[4], a1[4];
#pragma unroll
    for (int hh = 0; hh < 4; ++hh) {
        int h = wv * 4 + hh;
        u32x4 z4 = (u32x4){0u, 0u, 0u, 0u};
        u32x4 v0 = z4, v1 = z4;
        if (g < 3) {
            v0 = *(const u32x4*)(kvfrag + ((size_t)(h * 2 + 0) * 64 + lane) * 8);
            if (r16 < 5) v1 = *(const u32x4*)(kvfrag + ((size_t)(h * 2 + 1) * 64 + lane) * 8);
        }
        a0[hh] = __builtin_bit_cast(s16x8, v0);
        a1[hh] = __builtin_bit_cast(s16x8, v1);
    }

    const int w_ins0 = wv < 4 ? wv * 3 : 12 + (wv - 4) * 2;
    int w_src[3];
#pragma unroll
    for (int i = 0; i < 3; ++i) {
        int c = (w_ins0 + i) * 64 + lane;
        if (c >= 1280) c = 0;
        int krow = c / 10, cc = c % 10;
        w_src[i] = krow * D_ + col0 + cc * 8;
    }
    auto stage = [&](int slot, int ch) {
        char* dst = wbuf + slot * K2_CH_BYTES;
#pragma unroll
        for (int i = 0; i < 2; ++i)
            __builtin_amdgcn_global_load_lds((const unsigned*)(wo + w_src[i] + ch * 128 * D_),
                                             (unsigned*)(dst + (w_ins0 + i) * 1024 + lane * 16), 16, 0, 0);
        if (wv < 4)
            __builtin_amdgcn_global_load_lds((const unsigned*)(wo + w_src[2] + ch * 128 * D_),
                                             (unsigned*)(dst + (w_ins0 + 2) * 1024 + lane * 16), 16, 0, 0);
    };
    WAIT_V(0);
    BAR();
    STAMP(2,8);
    stage(0, 0); stage(1, 1); stage(2, 2);

    const f32x4 zero4 = (f32x4){0.f, 0.f, 0.f, 0.f};
#pragma unroll
    for (int hh = 0; hh < 4; ++hh) {
        int h = wv * 4 + hh;
        s16x8 bq[4];
#pragma unroll
        for (int st = 0; st < 4; ++st) {
            u32x4 v = (u32x4){0u, 0u, 0u, 0u};
            int row = st * 16 + r16;
            if (g < 3) {
                u32x2 lo = *(const u32x2*)(oimg + oimg_off(row, h * 20 + 8 * g));
                v[0] = lo[0]; v[1] = lo[1];
                if (g < 2) { u32x2 hi = *(const u32x2*)(oimg + oimg_off(row, h * 20 + 8 * g + 4)); v[2] = hi[0]; v[3] = hi[1]; }
            }
            bq[st] = __builtin_bit_cast(s16x8, v);
        }
#pragma unroll
        for (int st = 0; st < 4; ++st) {
            f32x4 d1 = __builtin_amdgcn_mfma_f32_16x16x32_bf16(a0[hh], bq[st], zero4, 0, 0, 0);
            f32x4 d2 = __builtin_amdgcn_mfma_f32_16x16x32_bf16(a1[hh], bq[st], zero4, 0, 0, 0);
            float den = __shfl(d2[0], 16 + r16);
            den = rbf(rbf(den) + 1e-6f);
            float inv = 1.0f / den;
            int row = st * 16 + r16;
            u32x2 v;
            v[0] = pack2(rbf(d1[0]) * inv, rbf(d1[1]) * inv);
            v[1] = pack2(rbf(d1[2]) * inv, rbf(d1[3]) * inv);
            *(u32x2*)(oimg + oimg_off(row, h * 20 + 4 * g)) = v;
            if (g == 0) {
                u32x2 v2;
                v2[0] = pack2(rbf(d2[0]) * inv, rbf(d2[1]) * inv);
                v2[1] = pack2(rbf(d2[2]) * inv, rbf(d2[3]) * inv);
                *(u32x2*)(oimg + oimg_off(row, h * 20 + 16)) = v2;
            }
        }
    }
    STAMP(2,1);

    const int wr = wv & 3, wc = wv >> 2;
    f32x4 acc[3];
#pragma unroll
    for (int n = 0; n < 3; ++n) acc[n] = zero4;
    const int q4 = (lane & 15) >> 2, p4 = lane & 3;
    const int w_off0 = (8 * g + q4) * 160 + p4 * 8 + wc * 96;
    const int orow = wr * 16 + r16;
    const int o_off0 = orow * 128 + ((g ^ (orow & 7)) << 4);
    const int o_off1 = orow * 128 + (((4 + g) ^ (orow & 7)) << 4);

#pragma unroll
    for (int ch = 0; ch < 5; ++ch) {
        if (wv < 4) { if (ch == 0) WAIT_V(6); else if (ch < 4) WAIT_V(3); else WAIT_V(0); }
        else        { if (ch == 0) WAIT_V(4); else if (ch < 4) WAIT_V(2); else WAIT_V(0); }
        WAIT_L(0);
        BAR();
        STAMP(2,2+ch);
        if (ch >= 1 && ch + 2 < 5) stage((ch + 2) % 3, ch + 2);
        const char* wb = wbuf + (ch % 3) * K2_CH_BYTES;
#pragma unroll
        for (int ks = 0; ks < 4; ++ks) {
            s16x8 of = *(const s16x8*)(oimg + (ch * 2 + (ks >> 1)) * 8192 + ((ks & 1) ? o_off1 : o_off0));
#pragma unroll
            for (int n = 0; n < 3; ++n) {
                if (n == 2 && wc == 1) continue;
                const char* p = wb + w_off0 + (32 * ks) * 160 + n * 32;
                s16x8 wf = cat8(tr_read(p), tr_read(p + 4 * 160));
                acc[n] = __builtin_amdgcn_mfma_f32_16x16x32_bf16(wf, of, acc[n], 0, 0, 0);
            }
        }
    }
    STAMP(2,7);
    int s = row0 + orow;
#pragma unroll
    for (int n = 0; n < 3; ++n) {
        if (n == 2 && wc == 1) continue;
        u32x2 v;
        v[0] = pack2(acc[n][0], acc[n][1]);
        v[1] = pack2(acc[n][2], acc[n][3]);
        *(u32x2*)(out + (size_t)s * D_ + col0 + (wc * 3 + n) * 16 + 4 * g) = v;
    }
}

extern "C" void kernel_launch(void* const* d_in, const int* in_sizes, int n_in,
                              void* d_out, int out_size, void* d_ws, size_t ws_size,
                              hipStream_t stream) {
    const u16* x = (const u16*)d_in[0];
    const u16* wqkv = (const u16*)d_in[1];
    const u16* wo = (const u16*)d_in[2];
    u16* out = (u16*)d_out;
    char* ws = (char*)d_ws;
    u16* pq = (u16*)ws;
    float* part = (float*)(ws + 4u * 1024 * 1024);
    u16* kvfrag = (u16*)(ws + 7u * 1024 * 1024);
    k1_kernel<<<192, 512, 0, stream>>>(x, wqkv, pq, part);
    kr_kernel<<<32, 512, 0, stream>>>(part, kvfrag);
    k2_kernel<<<256, 512, 0, stream>>>(pq, kvfrag, wo, out);
}
```

```cpp
#include <hip/hip_runtime.h>
#include <hip/hip_bf16.h>
#include <stdint.h>

typedef unsigned short u16;
typedef __attribute__((ext_vector_type(8))) short s16x8;
typedef __attribute__((ext_vector_type(4))) short s16x4;
typedef __attribute__((ext_vector_type(4))) float f32x4;
typedef __attribute__((ext_vector_type(2))) unsigned int u32x2;
typedef __attribute__((ext_vector_type(4))) unsigned int u32x4;

constexpr int S_ = 2048, D_ = 640, H_ = 32, DH_ = 20, N3_ = 1920;
constexpr int NRT = 16;
constexpr int PSZ = 13 * 256 + 80;

#define LDS_AS __attribute__((address_space(3)))

#define STAMP(k,i)

__device__ __forceinline__ float bf2f(u16 u) { return __uint_as_float(((unsigned)u) << 16); }
__device__ __forceinline__ u16 f2bf(float f) { __bf16 b = (__bf16)f; return __builtin_bit_cast(u16, b); }
__device__ __forceinline__ float rbf(float f) { return bf2f(f2bf(f)); }
__device__ __forceinline__ unsigned pack2(float a, float b) { return (unsigned)f2bf(a) | ((unsigned)f2bf(b) << 16); }
__device__ __forceinline__ float phi(float acc) {
    float t = rbf(acc);
    float p = t > 0.f ? t + 1.0f : __expf(t);
    return rbf(rbf(p) + 1e-6f);
}

#define WAIT_V(n) asm volatile("s_waitcnt vmcnt(" #n ")" ::: "memory")
#define WAIT_L(n) asm volatile("s_waitcnt lgkmcnt(" #n ")" ::: "memory")
#define BAR() __builtin_amdgcn_s_barrier()

__device__ __forceinline__ s16x4 tr_read(const char* lds_ptr) {
    return __builtin_amdgcn_ds_read_tr16_b64_v4i16((LDS_AS s16x4*)lds_ptr);
}
__device__ __forceinline__ s16x8 cat8(s16x4 a, s16x4 b) {
    s16x8 r; r[0]=a[0]; r[1]=a[1]; r[2]=a[2]; r[3]=a[3]; r[4]=b[0]; r[5]=b[1]; r[6]=b[2]; r[7]=b[3]; return r;
}

constexpr int K1_A_BYTES = 128 * 64 * 2;
constexpr int K1_B_BYTES = 64 * 160 * 2;
constexpr int K1_STAGE = K1_A_BYTES + K1_B_BYTES;
constexpr int K1_NST = 4;

template <bool IS_Q>
__device__ __forceinline__ void k1_body(const u16* __restrict__ x, const u16* __restrict__ w,
                                        u16* __restrict__ pq, float* __restrict__ part,
                                        char* smem, int rt, int cg) {
    const int tid = threadIdx.x, lane = tid & 63;
    const int wv = __builtin_amdgcn_readfirstlane(tid >> 6);
    const int wr = wv & 3, wc = wv >> 2;
    const int g = lane >> 4, r16 = lane & 15;
    const int row0 = rt * 128;

    int a_src[2], b_src[3];
#pragma unroll
    for (int i = 0; i < 2; ++i) {
        int ins = wv * 2 + i;
        int row = ins * 8 + (lane >> 3), slot = lane & 7;
        a_src[i] = (row0 + row) * D_ + ((slot ^ (row & 7)) * 8);
    }
    const int b_ins0 = wv < 4 ? wv * 3 : 12 + (wv - 4) * 2;
#pragma unroll
    for (int i = 0; i < 3; ++i) {
        int c = (b_ins0 + i) * 64 + lane;
        if (c >= 1280) c = 0;
        int krow = c / 20, cc = c % 20;
        int sc = cc ^ (((krow >> 3) & 1) << 1);
        int col = sc * 8;
        int gcol;
        if (IS_Q) gcol = cg * 160 + col;
        else gcol = (col < 80) ? (640 + (cg - 4) * 80 + col) : (1280 + (cg - 4) * 80 + (col - 80));
        b_src[i] = krow * N3_ + gcol;
    }

    auto stage = [&](int st, int kt) {
        char* a_dst = smem + st * K1_STAGE;
        char* b_dst = a_dst + K1_A_BYTES;
#pragma unroll
        for (int i = 0; i < 2; ++i)
            __builtin_amdgcn_global_load_lds((const unsigned*)(x + a_src[i] + kt * 64),
                                             (unsigned*)(a_dst + (wv * 2 + i) * 1024 + lane * 16), 16, 0, 0);
#pragma unroll
        for (int i = 0; i < 2; ++i)
            __builtin_amdgcn_global_load_lds((const unsigned*)(w + b_src[i] + kt * 64 * N3_),
                                             (unsigned*)(b_dst + (b_ins0 + i) * 1024 + lane * 16), 16, 0, 0);
        if (wv < 4)
            __builtin_amdgcn_global_load_lds((const unsigned*)(w + b_src[2] + kt * 64 * N3_),
                                             (unsigned*)(b_dst + (b_ins0 + 2) * 1024 + lane * 16), 16, 0, 0);
    };

    f32x4 acc[2][5];
#pragma unroll
    for (int m = 0; m < 2; ++m)
#pragma unroll
        for (int n = 0; n < 5; ++n) acc[m][n] = (f32x4){0.f, 0.f, 0.f, 0.f};

    STAMP(0,0);
    unsigned pf0, pf1;
    {
        const int jl = cg % 6, rl = rt & 3;
        const u16* pa[2];
#pragma unroll
        for (int i = 0; i < 2; ++i) {
            if (wv < 2) {
                int idx = jl * 224 + (wv * 2 + i) * 64 + lane;
                idx = idx > 1279 ? 1279 : idx;
                pa[i] = x + (size_t)(row0 + idx / 10) * D_ + (idx % 10) * 64;
            } else {
                int t = (wv < 7 ? wv - 2 : 4) * 2 + i;
                int idx = t * 64 + lane;
                int krow = rl * 160 + (idx >> 2), seg = idx & 3;
                int col;
                if (IS_Q) col = cg * 160 + (seg == 3 ? 159 : seg * 64);
                else col = ((seg & 2) ? 1280 : 640) + (cg - 4) * 80 + ((seg & 1) ? 79 : 0);
                pa[i] = w + (size_t)krow * N3_ + col;
            }
        }
        pf0 = *(const volatile u16*)pa[0];
        pf1 = *(const volatile u16*)pa[1];
    }
    stage(0, 0); stage(1, 1); stage(2, 2);

    int a_off[2][2];
#pragma unroll
    for (int m = 0; m < 2; ++m)
#pragma unroll
        for (int ks = 0; ks < 2; ++ks) {
            int row = wr * 32 + m * 16 + r16;
            a_off[m][ks] = row * 128 + (((4 * ks + g) ^ (row & 7)) * 16);
        }
    const int q4 = (lane & 15) >> 2, p4 = lane & 3;
    int b_off[5];
#pragma unroll
    for (int n = 0; n < 5; ++n) b_off[n] = (8 * g + q4) * 320 + p4 * 8 + (((wc * 5 + n) ^ (g & 1)) * 32);

#pragma unroll 1
    for (int kt = 0; kt < 10; ++kt) {
        if (kt < 8) { if (wv < 4) WAIT_V(10); else WAIT_V(8); }
        else WAIT_V(0);
        BAR();
        STAMP(0,1+kt);
        if (kt + 3 < 10) stage((kt + 3) & 3, kt + 3);
        const char* a_base = smem + (kt & 3) * K1_STAGE;
        const char* b_base = a_base + K1_A_BYTES;
#pragma unroll
        for (int ks = 0; ks < 2; ++ks) {
            s16x8 af[2];
#pragma unroll
            for (int m = 0; m < 2; ++m) af[m] = *(const s16x8*)(a_base + a_off[m][ks]);
#pragma unroll
            for (int n = 0; n < 5; ++n) {
                const char* p = b_base + b_off[n] + (32 * ks) * 320;
                s16x8 bfr = cat8(tr_read(p), tr_read(p + 4 * 320));
#pragma unroll
                for (int m = 0; m < 2; ++m) {
                    if (IS_Q) acc[m][n] = __builtin_amdgcn_mfma_f32_16x16x32_bf16(bfr, af[m], acc[m][n], 0, 0, 0);
                    else      acc[m][n] = __builtin_amdgcn_mfma_f32_16x16x32_bf16(af[m], bfr, acc[m][n], 0, 0, 0);
                }
            }
        }
    }
    STAMP(0,11);
    asm volatile("" :: "v"(pf0), "v"(pf1));

    if (IS_Q) {
#pragma unroll
        for (int m = 0; m < 2; ++m) {
            int s = row0 + wr * 32 + m * 16 + r16;
#pragma unroll
            for (int n = 0; n < 5; ++n) {
                u32x2 v;
                v[0] = pack2(phi(acc[m][n][0]), phi(acc[m][n][1]));
                v[1] = pack2(phi(acc[m][n][2]), phi(acc[m][n][3]));
                *(u32x2*)(pq + (size_t)s * D_ + cg * 160 + wc * 80 + n * 16 + 4 * g) = v;
            }
        }
    } else {
        s16x8 fr[5];
#pragma unroll
        for (int n = 0; n < 5; ++n) {
#pragma unroll
            for (int j = 0; j < 4; ++j) {
                float a0 = acc[0][n][j], a1 = acc[1][n][j];
                if (wc == 0) { a0 = phi(a0); a1 = phi(a1); }
                fr[n][j] = (short)f2bf(a0);
                fr[n][4 + j] = (short)f2bf(a1);
            }
        }
        BAR();
        char* xch = smem + 64 * 1024;
        if (wc == 1) {
#pragma unroll
            for (int n = 0; n < 5; ++n) *(s16x8*)(xch + ((wr * 5 + n) * 64 + lane) * 16) = fr[n];
        }
        __syncthreads();
        if (wc == 0) {
            s16x8 vf[5];
#pragma unroll
            for (int n = 0; n < 5; ++n) vf[n] = *(const s16x8*)(xch + ((wr * 5 + n) * 64 + lane) * 16);
            s16x8 ones;
#pragma unroll
            for (int j = 0; j < 8; ++j) ones[j] = (short)0x3F80;
            float* red = (float*)smem + wr * PSZ;
            const f32x4 zero4 = (f32x4){0.f, 0.f, 0.f, 0.f};
            int pi = 0;
#pragma unroll
            for (int kt2 = 0; kt2 < 5; ++kt2) {
#pragma unroll
                for (int dv = -1; dv <= 1; ++dv) {
                    int vt = kt2 + dv;
                    if (vt < 0 || vt > 4) continue;
                    f32x4 dd = __builtin_amdgcn_mfma_f32_16x16x32_bf16(fr[kt2], vf[vt], zero4, 0, 0, 0);
#pragma unroll
                    for (int r = 0; r < 4; ++r) red[pi * 256 + (4 * g + r) * 16 + r16] = dd[r];
                    ++pi;
                }
                f32x4 zz = __builtin_amdgcn_mfma_f32_16x16x32_bf16(fr[kt2], ones, zero4, 0, 0, 0);
                if (r16 == 0) *(f32x4*)(red + 13 * 256 + kt2 * 16 + 4 * g) = zz;
            }
        }
        __syncthreads();
        const float* rb = (const float*)smem;
        float* dst = part + ((size_t)rt * 8 + (cg - 4)) * PSZ;
        for (int i = tid; i < PSZ; i += 512)
            dst[i] = (rb[i] + rb[PSZ + i]) + (rb[2 * PSZ + i] + rb[3 * PSZ + i]);
    }
    STAMP(0,12);
}

__global__ __launch_bounds__(512) void k1_kernel(const u16* __restrict__ x, const u16* __restrict__ w,
                                                 u16* __restrict__ pq, float* __restrict__ part,
                                                 unsigned* __restrict__ cnt) {
    __shared__ __attribute__((aligned(16))) char smem[K1_NST * K1_STAGE];
    int b = blockIdx.x;
    if (b == 0 && threadIdx.x == 0) *cnt = 0u;
    int xcd = b & 7, idx = b >> 3;
    int rt = (xcd >> 1) * 4 + (idx & 3);
    int cg = (xcd & 1) * 6 + (idx >> 2);
    if (cg < 4) k1_body<true>(x, w, pq, part, smem, rt, cg);
    else        k1_body<false>(x, w, pq, part, smem, rt, cg);
}

constexpr int OST = 648;
constexpr int K2_O_BYTES = 64 * OST * 2;
constexpr int K2_CH_BYTES = 128 * 80 * 2;
typedef __attribute__((ext_vector_type(16))) float f32x16;

__global__ __launch_bounds__(512) void k2_kernel(const u16* __restrict__ pq, const float* __restrict__ part,
                                                 u16* kvfrag, unsigned* cnt,
                                                 const u16* __restrict__ wo, u16* __restrict__ out) {
    __shared__ __attribute__((aligned(16))) char smem[K2_O_BYTES + 3 * K2_CH_BYTES];
    const int tid = threadIdx.x, lane = tid & 63;
    const int wv = __builtin_amdgcn_readfirstlane(tid >> 6);
    const int g = lane >> 4, r16 = lane & 15;
    const int b = blockIdx.x;
    const int xcd = b & 7, idx = b >> 3;
    const int rt = xcd * 4 + (idx & 3), ct = idx >> 2;
    const int row0 = rt * 64, col0 = ct * 80;
    char* oimg = smem;
    char* wbuf = smem + K2_O_BYTES;

    STAMP(2,0);
    u32x4 pv[10];
#pragma unroll
    for (int j = 0; j < 10; ++j) {
        int c = j * 512 + tid;
        int row = c / 80, cc = c % 80;
        pv[j] = *(const u32x4*)(pq + (size_t)(row0 + row) * D_ + cc * 8);
    }

    if (b < 32) {
        float* red = (float*)wbuf;
        const int h = b;
        if (tid < 420) {
            int grp = h >> 2, hh = h & 3;
            int pidx;
            if (tid < 400) {
                int d = hh * 20 + tid / 20, e = hh * 20 + tid % 20;
                int kt = d >> 4, vt = e >> 4;
                int pi = kt == 0 ? vt : (3 * kt - 1 + (vt - kt + 1));
                pidx = pi * 256 + (d & 15) * 16 + (e & 15);
            } else {
                pidx = 13 * 256 + hh * 20 + (tid - 400);
            }
            float v[NRT];
#pragma unroll
            for (int p = 0; p < NRT; ++p) v[p] = part[((size_t)p * 8 + grp) * PSZ + pidx];
            float s = 0.f;
#pragma unroll
            for (int p = 0; p < NRT; ++p) s += v[p];
            red[tid] = rbf(s);
        }
        __syncthreads();
        if (tid < 128) {
            int ks = tid >> 6, row = lane & 31;
            s16x8 o;
#pragma unroll
            for (int j = 0; j < 8; ++j) {
                int k = 16 * ks + 8 * (lane >> 5) + j;
                float v = 0.f;
                if (k < 20) {
                    if (row < 20) v = red[k * 20 + row];
                    else if (row == 20) v = red[400 + k];
                }
                o[j] = (short)f2bf(v);
            }
            *(s16x8*)(kvfrag + ((size_t)(h * 2 + ks) * 64 + lane) * 8) = o;
        }
        WAIT_V(0);
        __syncthreads();
        if (tid == 0) {
            __builtin_amdgcn_fence(__ATOMIC_RELEASE, "agent");
            WAIT_V(0);
            __hip_atomic_fetch_add(cnt, 1u, __ATOMIC_RELAXED, __HIP_MEMORY_SCOPE_AGENT);
        }
    }

#pragma unroll
    for (int j = 0; j < 10; ++j) {
        int c = j * 512 + tid;
        int row = c / 80, cc = c % 80;
        *(u32x4*)(oimg + row * (OST * 2) + cc * 16) = pv[j];
    }
    if (tid < 64) *(u32x4*)(oimg + tid * (OST * 2) + 1280) = (u32x4){0u, 0u, 0u, 0u};

    const int w_ins0 = wv < 4 ? wv * 3 : 12 + (wv - 4) * 2;
    int w_src[3];
#pragma unroll
    for (int i = 0; i < 3; ++i) {
        int c = (w_ins0 + i) * 64 + lane;
        if (c >= 1280) c = 0;
        int krow = c / 10, cc = c % 10;
        w_src[i] = krow * D_ + col0 + cc * 8;
    }
    auto stage = [&](int slot, int ch) {
        char* dst = wbuf + slot * K2_CH_BYTES;
#pragma unroll
        for (int i = 0; i < 2; ++i)
            __builtin_amdgcn_global_load_lds((const unsigned*)(wo + w_src[i] + ch * 128 * D_),
                                             (unsigned*)(dst + (w_ins0 + i) * 1024 + lane * 16), 16, 0, 0);
        if (wv < 4)
            __builtin_amdgcn_global_load_lds((const unsigned*)(wo + w_src[2] + ch * 128 * D_),
                                             (unsigned*)(dst + (w_ins0 + 2) * 1024 + lane * 16), 16, 0, 0);
    };
    stage(0, 0); stage(1, 1); stage(2, 2);

    if (tid == 0) {
        unsigned spins = 0;
        while (__hip_atomic_load(cnt, __ATOMIC_RELAXED, __HIP_MEMORY_SCOPE_AGENT) < 32u) {
            __builtin_amdgcn_s_sleep(2);
            if (++spins > (1u << 24)) break;
        }
        __builtin_amdgcn_fence(__ATOMIC_ACQUIRE, "agent");
        WAIT_V(0);
    }
    WAIT_L(0);
    BAR();
    STAMP(2,8);

    s16x8 a0[4], a1[4];
    {
        const int row = lane & 31;
#pragma unroll
        for (int hh = 0; hh < 4; ++hh) {
            int h = wv * 4 + hh;
            u32x4 v0 = (u32x4){0u, 0u, 0u, 0u}, v1 = (u32x4){0u, 0u, 0u, 0u};
            if (row <= 20) {
                v0 = *(const volatile u32x4*)(kvfrag + ((size_t)(h * 2 + 0) * 64 + lane) * 8);
                if (lane < 32) v1 = *(const volatile u32x4*)(kvfrag + ((size_t)(h * 2 + 1) * 64 + lane) * 8);
            }
            a0[hh] = __builtin_bit_cast(s16x8, v0);
            a1[hh] = __builtin_bit_cast(s16x8, v1);
        }
    }

    {
        const int hl = lane >> 5;
        char* rbase = oimg + (lane & 31) * (OST * 2) + wv * 160 + hl * 16;
        char* wbase = oimg + (lane & 31) * (OST * 2) + wv * 160 + hl * 8;
        char* dummy = wbuf + 3 * K2_CH_BYTES - 8;
        f32x16 zero16;
#pragma unroll
        for (int i = 0; i < 16; ++i) zero16[i] = 0.f;
#pragma unroll
        for (int hh = 0; hh < 4; ++hh) {
#pragma unroll
            for (int sh = 0; sh < 2; ++sh) {
                const char* rp = rbase + sh * 32 * (OST * 2) + hh * 40;
                u32x2 l0 = *(const u32x2*)(rp), l1 = *(const u32x2*)(rp + 8);
                u32x2 l2 = *(const u32x2*)(rp + 32), l3 = *(const u32x2*)(rp + 40);
                u32x4 b0 = (u32x4){l0[0], l0[1], l1[0], l1[1]};
                u32x4 b1 = (u32x4){l2[0], l2[1], l3[0], l3[1]};
                f32x16 dd = __builtin_amdgcn_mfma_f32_32x32x16_bf16(a0[hh], __builtin_bit_cast(s16x8, b0), zero16, 0, 0, 0);
                dd = __builtin_amdgcn_mfma_f32_32x32x16_bf16(a1[hh], __builtin_bit_cast(s16x8, b1), dd, 0, 0, 0);
                unsigned du = __float_as_uint(dd[8]);
                auto sw = __builtin_amdgcn_permlane32_swap(du, du, false, false);
                float den = __uint_as_float(sw[1]);
                den = rbf(rbf(den) + 1e-6f);
                float inv = __builtin_amdgcn_rcpf(den);
                char* wp = wbase + sh * 32 * (OST * 2) + hh * 40;
#pragma unroll
                for (int q = 0; q < 3; ++q) {
                    u32x2 v;
                    v[0] = pack2(rbf(dd[4 * q + 0]) * inv, rbf(dd[4 * q + 1]) * inv);
                    v[1] = pack2(rbf(dd[4 * q + 2]) * inv, rbf(dd[4 * q + 3]) * inv);
                    if (q < 2) *(u32x2*)(wp + q * 16) = v;
                    else if (hl == 0) *(u32x2*)(wp + 32) = v;
                }
            }
        }
        (void)dummy;
    }
    STAMP(2,1);

    const int wr = wv & 3, wc = wv >> 2;
    const f32x4 zero4 = (f32x4){0.f, 0.f, 0.f, 0.f};
    f32x4 acc[3];
#pragma unroll
    for (int n = 0; n < 3; ++n) acc[n] = zero4;
    const int q4 = (lane & 15) >> 2, p4 = lane & 3;
    const int w_off0 = (8 * g + q4) * 160 + p4 * 8 + wc * 96;
    const int orow = wr * 16 + r16;
    const char* obase = oimg + orow * (OST * 2) + g * 16;

#pragma unroll
    for (int ch = 0; ch < 5; ++ch) {
        if (wv < 4) { if (ch == 0) WAIT_V(6); else if (ch < 4) WAIT_V(3); else WAIT_V(0); }
        else        { if (ch == 0) WAIT_V(4); else if (ch < 4) WAIT_V(2); else WAIT_V(0); }
        WAIT_L(0);
        BAR();
        STAMP(2,2+ch);
        if (ch >= 1 && ch + 2 < 5) stage((ch + 2) % 3, ch + 2);
        const char* wb = wbuf + (ch % 3) * K2_CH_BYTES;
#pragma unroll
        for (int ks = 0; ks < 4; ++ks) {
            s16x8 of = *(const s16x8*)(obase + ch * 256 + ks * 64);
#pragma unroll
            for (int n = 0; n < 3; ++n) {
                if (n == 2 && wc == 1) continue;
                const char* p = wb + w_off0 + (32 * ks) * 160 + n * 32;
                s16x8 wf = cat8(tr_read(p), tr_read(p + 4 * 160));
                acc[n] = __builtin_amdgcn_mfma_f32_16x16x32_bf16(wf, of, acc[n], 0, 0, 0);
            }
        }
    }
    STAMP(2,7);
    int s = row0 + orow;
#pragma unroll
    for (int n = 0; n < 3; ++n) {
        if (n == 2 && wc == 1) continue;
        u32x2 v;
        v[0] = pack2(acc[n][0], acc[n][1]);
        v[1] = pack2(acc[n][2], acc[n][3]);
        *(u32x2*)(out + (size_t)s * D_ + col0 + (wc * 3 + n) * 16 + 4 * g) = v;
    }
}

extern "C" void kernel_launch(void* const* d_in, const int* in_sizes, int n_in,
                              void* d_out, int out_size, void* d_ws, size_t ws_size,
                              hipStream_t stream) {
    const u16* x = (const u16*)d_in[0];
    const u16* wqkv = (const u16*)d_in[1];
    const u16* wo = (const u16*)d_in[2];
    u16* out = (u16*)d_out;
    char* ws = (char*)d_ws;
    u16* pq = (u16*)ws;
    float* part = (float*)(ws + 4u * 1024 * 1024);
    u16* kvfrag = (u16*)(ws + 7u * 1024 * 1024);
    unsigned* cnt = (unsigned*)(ws + 8u * 1024 * 1024);
    k1_kernel<<<192, 512, 0, stream>>>(x, wqkv, pq, part, cnt);
    k2_kernel<<<256, 512, 0, stream>>>(pq, part, kvfrag, cnt, wo, out);
}
```

```cpp
#include <hip/hip_runtime.h>
#include <hip/hip_bf16.h>
#include <stdint.h>

typedef unsigned short u16;
typedef __attribute__((ext_vector_type(8))) short s16x8;
typedef __attribute__((ext_vector_type(4))) short s16x4;
typedef __attribute__((ext_vector_type(4))) float f32x4;
typedef __attribute__((ext_vector_type(2))) unsigned int u32x2;
typedef __attribute__((ext_vector_type(4))) unsigned int u32x4;

constexpr int S_ = 2048, D_ = 640, H_ = 32, DH_ = 20, N3_ = 1920;
constexpr int NRT = 16;
constexpr int PSZ = 13 * 256 + 80;

#define LDS_AS __attribute__((address_space(3)))

#define STAMP(k,i)

__device__ __forceinline__ float bf2f(u16 u) { return __uint_as_float(((unsigned)u) << 16); }
__device__ __forceinline__ u16 f2bf(float f) { __bf16 b = (__bf16)f; return __builtin_bit_cast(u16, b); }
__device__ __forceinline__ float rbf(float f) { return bf2f(f2bf(f)); }
__device__ __forceinline__ unsigned pack2(float a, float b) { return (unsigned)f2bf(a) | ((unsigned)f2bf(b) << 16); }
__device__ __forceinline__ float phi(float acc) {
    float t = rbf(acc);
    float p = t > 0.f ? t + 1.0f : __expf(t);
    return rbf(rbf(p) + 1e-6f);
}

#define WAIT_V(n) asm volatile("s_waitcnt vmcnt(" #n ")" ::: "memory")
#define WAIT_L(n) asm volatile("s_waitcnt lgkmcnt(" #n ")" ::: "memory")
#define BAR() __builtin_amdgcn_s_barrier()

__device__ __forceinline__ s16x4 tr_read(const char* lds_ptr) {
    return __builtin_amdgcn_ds_read_tr16_b64_v4i16((LDS_AS s16x4*)lds_ptr);
}
__device__ __forceinline__ s16x8 cat8(s16x4 a, s16x4 b) {
    s16x8 r; r[0]=a[0]; r[1]=a[1]; r[2]=a[2]; r[3]=a[3]; r[4]=b[0]; r[5]=b[1]; r[6]=b[2]; r[7]=b[3]; return r;
}

constexpr int K1_A_BYTES = 128 * 64 * 2;
constexpr int K1_B_BYTES = 64 * 160 * 2;
constexpr int K1_STAGE = K1_A_BYTES + K1_B_BYTES;
constexpr int K1_NST = 4;

template <bool IS_Q>
__device__ __forceinline__ void k1_body(const u16* __restrict__ x, const u16* __restrict__ w,
                                        u16* __restrict__ pq, float* __restrict__ part,
                                        char* smem, int rt, int cg) {
    const int tid = threadIdx.x, lane = tid & 63;
    const int wv = __builtin_amdgcn_readfirstlane(tid >> 6);
    const int wr = wv & 3, wc = (wv >> 2) & 1;
    const int g = lane >> 4, r16 = lane & 15;
    const int row0 = rt * 128;

    f32x4 acc[2][5];
#pragma unroll
    for (int m = 0; m < 2; ++m)
#pragma unroll
        for (int n = 0; n < 5; ++n) acc[m][n] = (f32x4){0.f, 0.f, 0.f, 0.f};

    STAMP(0,0);
    if (wv >= 8) {
        const int ld = wv - 8;
        int a_src[8], b_src[10];
#pragma unroll
        for (int i = 0; i < 8; ++i) {
            int row = (ld * 8 + i) * 8 + (lane >> 3), slot = lane & 7;
            a_src[i] = (row0 + row) * D_ + ((slot ^ (row & 7)) * 8);
        }
#pragma unroll
        for (int i = 0; i < 10; ++i) {
            int c = (ld * 10 + i) * 64 + lane;
            int krow = c / 20, cc = c % 20;
            int sc = cc ^ (((krow >> 3) & 1) << 1);
            int col = sc * 8;
            int gcol;
            if (IS_Q) gcol = cg * 160 + col;
            else gcol = (col < 80) ? (640 + (cg - 4) * 80 + col) : (1280 + (cg - 4) * 80 + (col - 80));
            b_src[i] = krow * N3_ + gcol;
        }
        auto stage = [&](int st, int kt) {
            char* a_dst = smem + st * K1_STAGE + ld * 8192 + lane * 16;
            char* b_dst = smem + st * K1_STAGE + K1_A_BYTES + ld * 10240 + lane * 16;
#pragma unroll
            for (int i = 0; i < 8; ++i)
                __builtin_amdgcn_global_load_lds((const unsigned*)(x + a_src[i] + kt * 64), (unsigned*)(a_dst + i * 1024), 16, 0, 0);
#pragma unroll
            for (int i = 0; i < 10; ++i)
                __builtin_amdgcn_global_load_lds((const unsigned*)(w + b_src[i] + kt * 64 * N3_), (unsigned*)(b_dst + i * 1024), 16, 0, 0);
        };
        stage(0, 0); stage(1, 1); stage(2, 2);
        WAIT_V(36);
        BAR();
#pragma unroll 1
        for (int kt = 0; kt < 10; ++kt) {
            if (kt < 7) WAIT_V(18); else WAIT_V(0);
            BAR();
            if (kt + 3 < 10) stage((kt + 3) & 3, kt + 3);
        }
    } else {
        int a_off[2][2];
#pragma unroll
        for (int m = 0; m < 2; ++m)
#pragma unroll
            for (int ks = 0; ks < 2; ++ks) {
                int row = wr * 32 + m * 16 + r16;
                a_off[m][ks] = row * 128 + (((4 * ks + g) ^ (row & 7)) * 16);
            }
        const int q4 = (lane & 15) >> 2, p4 = lane & 3;
        int b_off[5];
#pragma unroll
        for (int n = 0; n < 5; ++n) b_off[n] = (8 * g + q4) * 320 + p4 * 8 + (((wc * 5 + n) ^ (g & 1)) * 32);

        s16x8 af0[2], bq0[5], af1[2], bq1[5];
        auto rd = [&](s16x8* af, s16x8* bq, int kt, int ks) {
            const char* a_base = smem + (kt & 3) * K1_STAGE;
            const char* b_base = a_base + K1_A_BYTES;
#pragma unroll
            for (int m = 0; m < 2; ++m) af[m] = *(const s16x8*)(a_base + a_off[m][ks]);
#pragma unroll
            for (int n = 0; n < 5; ++n) {
                const char* p = b_base + b_off[n] + (32 * ks) * 320;
                bq[n] = cat8(tr_read(p), tr_read(p + 4 * 320));
            }
        };
        auto mm = [&](const s16x8* af, const s16x8* bq) {
#pragma unroll
            for (int n = 0; n < 5; ++n) {
#pragma unroll
                for (int m = 0; m < 2; ++m) {
                    if (IS_Q) acc[m][n] = __builtin_amdgcn_mfma_f32_16x16x32_bf16(bq[n], af[m], acc[m][n], 0, 0, 0);
                    else      acc[m][n] = __builtin_amdgcn_mfma_f32_16x16x32_bf16(af[m], bq[n], acc[m][n], 0, 0, 0);
                }
            }
        };
        BAR();
        rd(af0, bq0, 0, 0);
#pragma unroll 1
        for (int kt = 0; kt < 10; ++kt) {
            BAR();
            STAMP(0,1+kt);
            __builtin_amdgcn_s_waitcnt(0xC07F);
            __builtin_amdgcn_sched_barrier(0);
            rd(af1, bq1, kt, 1);
            __builtin_amdgcn_sched_barrier(0);
            mm(af0, bq0);
            __builtin_amdgcn_sched_barrier(0);
            rd(af0, bq0, kt + 1, 0);
            __builtin_amdgcn_sched_barrier(0);
            mm(af1, bq1);
            __builtin_amdgcn_sched_barrier(0);
        }
    }
    STAMP(0,11);

    if (IS_Q) {
        if (wv >= 8) return;
#pragma unroll
        for (int m = 0; m < 2; ++m) {
            int s = row0 + wr * 32 + m * 16 + r16;
#pragma unroll
            for (int n = 0; n < 5; ++n) {
                u32x2 v;
                v[0] = pack2(phi(acc[m][n][0]), phi(acc[m][n][1]));
                v[1] = pack2(phi(acc[m][n][2]), phi(acc[m][n][3]));
                *(u32x2*)(pq + (size_t)s * D_ + cg * 160 + wc * 80 + n * 16 + 4 * g) = v;
            }
        }
    } else {
        s16x8 fr[5];
#pragma unroll
        for (int n = 0; n < 5; ++n) {
#pragma unroll
            for (int j = 0; j < 4; ++j) {
                float a0 = acc[0][n][j], a1 = acc[1][n][j];
                if (wc == 0) { a0 = phi(a0); a1 = phi(a1); }
                fr[n][j] = (short)f2bf(a0);
                fr[n][4 + j] = (short)f2bf(a1);
            }
        }
        BAR();
        char* xch = smem + 64 * 1024;
        if (wc == 1 && wv < 8) {
#pragma unroll
            for (int n = 0; n < 5; ++n) *(s16x8*)(xch + ((wr * 5 + n) * 64 + lane) * 16) = fr[n];
        }
        __syncthreads();
        if (wv < 4) {
            s16x8 vf[5];
#pragma unroll
            for (int n = 0; n < 5; ++n) vf[n] = *(const s16x8*)(xch + ((wr * 5 + n) * 64 + lane) * 16);
            s16x8 ones;
#pragma unroll
            for (int j = 0; j < 8; ++j) ones[j] = (short)0x3F80;
            float* red = (float*)smem + wr * PSZ;
            const f32x4 zero4 = (f32x4){0.f, 0.f, 0.f, 0.f};
            int pi = 0;
#pragma unroll
            for (int kt2 = 0; kt2 < 5; ++kt2) {
#pragma unroll
                for (int dv = -1; dv <= 1; ++dv) {
                    int vt = kt2 + dv;
                    if (vt < 0 || vt > 4) continue;
                    f32x4 dd = __builtin_amdgcn_mfma_f32_16x16x32_bf16(fr[kt2], vf[vt], zero4, 0, 0, 0);
#pragma unroll
                    for (int r = 0; r < 4; ++r) red[pi * 256 + (4 * g + r) * 16 + r16] = dd[r];
                    ++pi;
                }
                f32x4 zz = __builtin_amdgcn_mfma_f32_16x16x32_bf16(fr[kt2], ones, zero4, 0, 0, 0);
                if (r16 == 0) *(f32x4*)(red + 13 * 256 + kt2 * 16 + 4 * g) = zz;
            }
        }
        __syncthreads();
        const float* rb = (const float*)smem;
        float* dst = part + ((size_t)rt * 8 + (cg - 4)) * PSZ;
        for (int i = tid; i < PSZ; i += 640)
            dst[i] = (rb[i] + rb[PSZ + i]) + (rb[2 * PSZ + i] + rb[3 * PSZ + i]);
    }
    STAMP(0,12);
}

__global__ __launch_bounds__(640) void k1_kernel(const u16* __restrict__ x, const u16* __restrict__ w,
                                                 u16* __restrict__ pq, float* __restrict__ part,
                                                 unsigned* __restrict__ cnt) {
    __shared__ __attribute__((aligned(16))) char smem[K1_NST * K1_STAGE];
    int b = blockIdx.x;
    if (b == 0 && threadIdx.x < 8) cnt[threadIdx.x * 64] = 0u;
    int xcd = b & 7, idx = b >> 3;
    int rt = (xcd >> 1) * 4 + (idx & 3);
    int cg = (xcd & 1) * 6 + (idx >> 2);
    if (cg < 4) k1_body<true>(x, w, pq, part, smem, rt, cg);
    else        k1_body<false>(x, w, pq, part, smem, rt, cg);
}

constexpr int OST = 648;
constexpr int K2_O_BYTES = 64 * OST * 2;
constexpr int K2_CH_BYTES = 128 * 80 * 2;
typedef __attribute__((ext_vector_type(16))) float f32x16;

__global__ __launch_bounds__(640) void k2_kernel(const u16* __restrict__ pq, const float* __restrict__ part,
                                                 u16* kvfrag, unsigned* cnt,
                                                 const u16* __restrict__ wo, u16* __restrict__ out) {
    __shared__ __attribute__((aligned(16))) char smem[K2_O_BYTES + 3 * K2_CH_BYTES];
    const int tid = threadIdx.x, lane = tid & 63;
    const int wv = __builtin_amdgcn_readfirstlane(tid >> 6);
    const int g = lane >> 4, r16 = lane & 15;
    const int b = blockIdx.x;
    const int xcd = b & 7, idx = b >> 3;
    const int rt = xcd * 4 + (idx & 3), ct = idx >> 2;
    const int row0 = rt * 64, col0 = ct * 80;
    char* oimg = smem;
    char* wbuf = smem + K2_O_BYTES;

    if (wv >= 8) {
        const int ld = wv - 8;
        if (b < 32) { __syncthreads(); __syncthreads(); __syncthreads(); }
#pragma unroll
        for (int ch = 0; ch < 3; ++ch)
#pragma unroll
            for (int i = 0; i < 10; ++i) {
                int c = (ld * 10 + i) * 64 + lane;
                const u16* src = wo + (size_t)(ch * 128 + c / 10) * D_ + col0 + (c % 10) * 8;
                __builtin_amdgcn_global_load_lds((const unsigned*)src,
                    (unsigned*)(wbuf + ch * K2_CH_BYTES + (ld * 10 + i) * 1024 + lane * 16), 16, 0, 0);
            }
        u32x4 hold[20];
#pragma unroll
        for (int i = 0; i < 20; ++i) {
            int c = i * 64 + lane;
            hold[i] = *(const u32x4*)(wo + (size_t)((3 + ld) * 128 + c / 10) * D_ + col0 + (c % 10) * 8);
        }
        WAIT_V(0);
        BAR();
        BAR();
        BAR();
        if (ld == 0) {
#pragma unroll
            for (int i = 0; i < 20; ++i) *(u32x4*)(wbuf + i * 1024 + lane * 16) = hold[i];
        }
        WAIT_L(0);
        BAR();
        if (ld == 1) {
#pragma unroll
            for (int i = 0; i < 20; ++i) *(u32x4*)(wbuf + K2_CH_BYTES + i * 1024 + lane * 16) = hold[i];
        }
        WAIT_L(0);
        BAR();
        BAR();
        return;
    }

    STAMP(2,0);
    float psum = 0.f;
    if (b < 32 && tid < 420) {
        int grp = b >> 2, hh = b & 3;
        int pidx;
        if (tid < 400) {
            int d = hh * 20 + tid / 20, e = hh * 20 + tid % 20;
            int kt = d >> 4, vt = e >> 4;
            int pi = kt == 0 ? vt : (3 * kt - 1 + (vt - kt + 1));
            pidx = pi * 256 + (d & 15) * 16 + (e & 15);
        } else {
            pidx = 13 * 256 + hh * 20 + (tid - 400);
        }
        float v[NRT];
#pragma unroll
        for (int p = 0; p < NRT; ++p) v[p] = part[((size_t)p * 8 + grp) * PSZ + pidx];
#pragma unroll
        for (int p = 0; p < NRT; ++p) psum += v[p];
    }
    u32x4 pv[10];
#pragma unroll
    for (int j = 0; j < 10; ++j) {
        int c = j * 512 + tid;
        int row = c / 80, cc = c % 80;
        pv[j] = *(const u32x4*)(pq + (size_t)(row0 + row) * D_ + cc * 8);
    }

    if (b < 32) {
        u16* red = (u16*)wbuf;
        if (tid < 128) *(u32x4*)(red + tid * 8) = (u32x4){0u, 0u, 0u, 0u};
        __syncthreads();
        if (tid < 400) red[(tid % 20) * 32 + tid / 20] = f2bf(psum);
        else if (tid < 420) red[20 * 32 + (tid - 400)] = f2bf(psum);
        __syncthreads();
        if (tid < 128) {
            int ks = tid >> 6;
            u32x4 ov = *(const u32x4*)(red + (lane & 31) * 32 + 16 * ks + 8 * (lane >> 5));
            unsigned long long* dst = (unsigned long long*)(kvfrag + ((size_t)(b * 2 + ks) * 64 + lane) * 8);
            __hip_atomic_store(dst, (unsigned long long)ov[0] | ((unsigned long long)ov[1] << 32), __ATOMIC_RELAXED, __HIP_MEMORY_SCOPE_AGENT);
            __hip_atomic_store(dst + 1, (unsigned long long)ov[2] | ((unsigned long long)ov[3] << 32), __ATOMIC_RELAXED, __HIP_MEMORY_SCOPE_AGENT);
        }
        WAIT_V(0);
        __syncthreads();
        if (tid < 8) __hip_atomic_fetch_add(cnt + tid * 64, 1u, __ATOMIC_RELAXED, __HIP_MEMORY_SCOPE_AGENT);
        STAMP(1,0);
    }

#pragma unroll
    for (int j = 0; j < 10; ++j) {
        int c = j * 512 + tid;
        int row = c / 80, cc = c % 80;
        *(u32x4*)(oimg + row * (OST * 2) + cc * 16) = pv[j];
    }
    if (tid < 64) *(u32x4*)(oimg + tid * (OST * 2) + 1280) = (u32x4){0u, 0u, 0u, 0u};

    if (tid == 0) {
        unsigned spins = 0;
        while (__hip_atomic_load(cnt + (b & 7) * 64, __ATOMIC_RELAXED, __HIP_MEMORY_SCOPE_AGENT) < 32u) {
            __builtin_amdgcn_s_sleep(4);
            if (++spins > (1u << 24)) break;
        }
        __builtin_amdgcn_fence(__ATOMIC_ACQUIRE, "wavefront");
    }
    WAIT_L(0);
    BAR();
    STAMP(2,8);

    s16x8 a0[4], a1[4];
    {
        const int row = lane & 31;
#pragma unroll
        for (int hh = 0; hh < 4; ++hh) {
            int h = wv * 4 + hh;
            u32x4 v0 = (u32x4){0u, 0u, 0u, 0u}, v1 = (u32x4){0u, 0u, 0u, 0u};
            if (row <= 20) {
                const unsigned long long* s0 = (const unsigned long long*)(kvfrag + ((size_t)(h * 2 + 0) * 64 + lane) * 8);
                unsigned long long t0 = __hip_atomic_load(s0, __ATOMIC_RELAXED, __HIP_MEMORY_SCOPE_AGENT);
                unsigned long long t1 = __hip_atomic_load(s0 + 1, __ATOMIC_RELAXED, __HIP_MEMORY_SCOPE_AGENT);
                v0 = (u32x4){(unsigned)t0, (unsigned)(t0 >> 32), (unsigned)t1, (unsigned)(t1 >> 32)};
                if (lane < 32) {
                    const unsigned long long* s1 = (const unsigned long long*)(kvfrag + ((size_t)(h * 2 + 1) * 64 + lane) * 8);
                    unsigned long long t2 = __hip_atomic_load(s1, __ATOMIC_RELAXED, __HIP_MEMORY_SCOPE_AGENT);
                    unsigned long long t3 = __hip_atomic_load(s1 + 1, __ATOMIC_RELAXED, __HIP_MEMORY_SCOPE_AGENT);
                    v1 = (u32x4){(unsigned)t2, (unsigned)(t2 >> 32), (unsigned)t3, (unsigned)(t3 >> 32)};
                }
            }
            a0[hh] = __builtin_bit_cast(s16x8, v0);
            a1[hh] = __builtin_bit_cast(s16x8, v1);
        }
    }
    STAMP(2,9);
    {
        const int hl = lane >> 5;
        char* rbase = oimg + (lane & 31) * (OST * 2) + wv * 160 + hl * 16;
        char* wbase = oimg + (lane & 31) * (OST * 2) + wv * 160 + hl * 8;
        f32x16 zero16;
#pragma unroll
        for (int i = 0; i < 16; ++i) zero16[i] = 0.f;
#pragma unroll
        for (int hh = 0; hh < 4; ++hh) {
#pragma unroll
            for (int sh = 0; sh < 2; ++sh) {
                const char* rp = rbase + sh * 32 * (OST * 2) + hh * 40;
                u32x2 l0 = *(const u32x2*)(rp), l1 = *(const u32x2*)(rp + 8);
                u32x2 l2 = *(const u32x2*)(rp + 32), l3 = *(const u32x2*)(rp + 40);
                u32x4 b0 = (u32x4){l0[0], l0[1], l1[0], l1[1]};
                u32x4 b1 = (u32x4){l2[0], l2[1], l3[0], l3[1]};
                f32x16 dd = __builtin_amdgcn_mfma_f32_32x32x16_bf16(a0[hh], __builtin_bit_cast(s16x8, b0), zero16, 0, 0, 0);
                dd = __builtin_amdgcn_mfma_f32_32x32x16_bf16(a1[hh], __builtin_bit_cast(s16x8, b1), dd, 0, 0, 0);
                unsigned du = __float_as_uint(dd[8]);
                auto sw = __builtin_amdgcn_permlane32_swap(du, du, false, false);
                float den = __uint_as_float(sw[1]);
                den = rbf(rbf(den) + 1e-6f);
                float inv = __builtin_amdgcn_rcpf(den);
                char* wp = wbase + sh * 32 * (OST * 2) + hh * 40;
#pragma unroll
                for (int q = 0; q < 3; ++q) {
                    u32x2 v;
                    v[0] = pack2(rbf(dd[4 * q + 0]) * inv, rbf(dd[4 * q + 1]) * inv);
                    v[1] = pack2(rbf(dd[4 * q + 2]) * inv, rbf(dd[4 * q + 3]) * inv);
                    if (q < 2) *(u32x2*)(wp + q * 16) = v;
                    else if (hl == 0) *(u32x2*)(wp + 32) = v;
                }
            }
        }
    }
    STAMP(2,1);

    const int wr = wv & 3, wc = wv >> 2;
    const f32x4 zero4 = (f32x4){0.f, 0.f, 0.f, 0.f};
    f32x4 acc[3];
#pragma unroll
    for (int n = 0; n < 3; ++n) acc[n] = zero4;
    const int q4 = (lane & 15) >> 2, p4 = lane & 3;
    const int w_off0 = (8 * g + q4) * 160 + p4 * 8 + wc * 96;
    const int orow = wr * 16 + r16;
    const char* obase = oimg + orow * (OST * 2) + g * 16;

#pragma unroll
    for (int ch = 0; ch < 5; ++ch) {
        WAIT_L(0);
        BAR();
        STAMP(2,2+ch);
        const char* wb = wbuf + (ch % 3) * K2_CH_BYTES;
        s16x8 of[4], wf[4][3];
#pragma unroll
        for (int ks = 0; ks < 4; ++ks) {
            of[ks] = *(const s16x8*)(obase + ch * 256 + ks * 64);
#pragma unroll
            for (int n = 0; n < 3; ++n) {
                if (n == 2 && wc == 1) continue;
                const char* p = wb + w_off0 + (32 * ks) * 160 + n * 32;
                wf[ks][n] = cat8(tr_read(p), tr_read(p + 4 * 160));
            }
        }
#pragma unroll
        for (int ks = 0; ks < 4; ++ks)
#pragma unroll
            for (int n = 0; n < 3; ++n) {
                if (n == 2 && wc == 1) continue;
                acc[n] = __builtin_amdgcn_mfma_f32_16x16x32_bf16(wf[ks][n], of[ks], acc[n], 0, 0, 0);
            }
    }
    STAMP(2,7);
    int s = row0 + orow;
#pragma unroll
    for (int n = 0; n < 3; ++n) {
        if (n == 2 && wc == 1) continue;
        u32x2 v;
        v[0] = pack2(acc[n][0], acc[n][1]);
        v[1] = pack2(acc[n][2], acc[n][3]);
        *(u32x2*)(out + (size_t)s * D_ + col0 + (wc * 3 + n) * 16 + 4 * g) = v;
    }
}

extern "C" void kernel_launch(void* const* d_in, const int* in_sizes, int n_in,
                              void* d_out, int out_size, void* d_ws, size_t ws_size,
                              hipStream_t stream) {
    const u16* x = (const u16*)d_in[0];
    const u16* wqkv = (const u16*)d_in[1];
    const u16* wo = (const u16*)d_in[2];
    u16* out = (u16*)d_out;
    char* ws = (char*)d_ws;
    u16* pq = (u16*)ws;
    float* part = (float*)(ws + 4u * 1024 * 1024);
    u16* kvfrag = (u16*)(ws + 7u * 1024 * 1024);
    unsigned* cnt = (unsigned*)(ws + 8u * 1024 * 1024);
    k1_kernel<<<192, 640, 0, stream>>>(x, wqkv, pq, part, cnt);
    k2_kernel<<<256, 640, 0, stream>>>(pq, part, kvfrag, cnt, wo, out);
}
```

```cpp
#include <hip/hip_runtime.h>
#include <hip/hip_bf16.h>
#include <stdint.h>
#include <type_traits>

typedef unsigned short u16;
typedef __attribute__((ext_vector_type(8))) short s16x8;
typedef __attribute__((ext_vector_type(4))) short s16x4;
typedef __attribute__((ext_vector_type(4))) float f32x4;
typedef __attribute__((ext_vector_type(2))) unsigned int u32x2;
typedef __attribute__((ext_vector_type(4))) unsigned int u32x4;

constexpr int S_ = 2048, D_ = 640, H_ = 32, DH_ = 20, N3_ = 1920;
constexpr int NRT = 16;
constexpr int PSZ = 13 * 256 + 80;

#define LDS_AS __attribute__((address_space(3)))

#define STAMP(k,i)

__device__ __forceinline__ float bf2f(u16 u) { return __uint_as_float(((unsigned)u) << 16); }
__device__ __forceinline__ u16 f2bf(float f) { __bf16 b = (__bf16)f; return __builtin_bit_cast(u16, b); }
__device__ __forceinline__ float rbf(float f) { return bf2f(f2bf(f)); }
__device__ __forceinline__ unsigned pack2(float a, float b) { return (unsigned)f2bf(a) | ((unsigned)f2bf(b) << 16); }
__device__ __forceinline__ float phi(float acc) {
    float t = rbf(acc);
    float p = t > 0.f ? t + 1.0f : __expf(t);
    return rbf(rbf(p) + 1e-6f);
}

#define WAIT_V(n) asm volatile("s_waitcnt vmcnt(" #n ")" ::: "memory")
#define WAIT_L(n) asm volatile("s_waitcnt lgkmcnt(" #n ")" ::: "memory")
#define BAR() __builtin_amdgcn_s_barrier()

__device__ __forceinline__ s16x4 tr_read(const char* lds_ptr) {
    return __builtin_amdgcn_ds_read_tr16_b64_v4i16((LDS_AS s16x4*)lds_ptr);
}
__device__ __forceinline__ s16x8 cat8(s16x4 a, s16x4 b) {
    s16x8 r; r[0]=a[0]; r[1]=a[1]; r[2]=a[2]; r[3]=a[3]; r[4]=b[0]; r[5]=b[1]; r[6]=b[2]; r[7]=b[3]; return r;
}

constexpr int K1_A_BYTES = 128 * 64 * 2;
constexpr int K1_B_BYTES = 64 * 160 * 2;
constexpr int K1_STAGE = K1_A_BYTES + K1_B_BYTES;
constexpr int K1_NST = 4;

template <bool IS_Q>
__device__ __forceinline__ void k1_body(const u16* __restrict__ x, const u16* __restrict__ w,
                                        u16* __restrict__ pq, float* __restrict__ part,
                                        char* smem, int rt, int cg) {
    const int tid = threadIdx.x, lane = tid & 63;
    const int wv = __builtin_amdgcn_readfirstlane(tid >> 6);
    const int kh = (wv >> 2) & 1, wr2 = (wv >> 1) & 1, wc = wv & 1;
    const int wr = wr2 * 2 + kh;
    const int g = lane >> 4, r16 = lane & 15;
    const int row0 = rt * 128;

    f32x4 acc4[4][5];
#pragma unroll
    for (int m = 0; m < 4; ++m)
#pragma unroll
        for (int n = 0; n < 5; ++n) acc4[m][n] = (f32x4){0.f, 0.f, 0.f, 0.f};

    STAMP(0,0);
    if (wv >= 8) {
        const int ld = wv - 8;
        int a_src[8], b_src[10];
#pragma unroll
        for (int i = 0; i < 8; ++i) {
            int row = (ld * 8 + i) * 8 + (lane >> 3), slot = lane & 7;
            a_src[i] = (row0 + row) * D_ + ((slot ^ (row & 7)) * 8);
        }
#pragma unroll
        for (int i = 0; i < 10; ++i) {
            int c = (ld * 10 + i) * 64 + lane;
            int krow = c / 20, cc = c % 20;
            int sc = cc ^ (((krow >> 3) & 1) << 1);
            int col = sc * 8;
            int gcol;
            if (IS_Q) gcol = cg * 160 + col;
            else gcol = (col < 80) ? (640 + (cg - 4) * 80 + col) : (1280 + (cg - 4) * 80 + (col - 80));
            b_src[i] = krow * N3_ + gcol;
        }
        auto stage = [&](int st, int kt) {
            char* a_dst = smem + st * K1_STAGE + ld * 8192 + lane * 16;
            char* b_dst = smem + st * K1_STAGE + K1_A_BYTES + ld * 10240 + lane * 16;
#pragma unroll
            for (int i = 0; i < 8; ++i)
                __builtin_amdgcn_global_load_lds((const unsigned*)(x + a_src[i] + kt * 64), (unsigned*)(a_dst + i * 1024), 16, 0, 0);
#pragma unroll
            for (int i = 0; i < 10; ++i)
                __builtin_amdgcn_global_load_lds((const unsigned*)(w + b_src[i] + kt * 64 * N3_), (unsigned*)(b_dst + i * 1024), 16, 0, 0);
        };
        stage(0, 0); stage(1, 1); stage(2, 2);
        WAIT_V(36);
        BAR();
#pragma unroll 1
        for (int kt = 0; kt < 10; ++kt) {
            if (kt < 7) WAIT_V(18); else WAIT_V(0);
            BAR();
            if (kt + 3 < 10) stage((kt + 3) & 3, kt + 3);
        }
    } else {
        int a_off[4];
#pragma unroll
        for (int m = 0; m < 4; ++m) {
            int row = wr2 * 64 + m * 16 + r16;
            a_off[m] = row * 128 + (((4 * kh + g) ^ (row & 7)) * 16);
        }
        const int q4 = (lane & 15) >> 2, p4 = lane & 3;
        int b_off[5];
#pragma unroll
        for (int n = 0; n < 5; ++n)
            b_off[n] = K1_A_BYTES + (32 * kh + 8 * g + q4) * 320 + p4 * 8 + (((wc * 5 + n) ^ (g & 1)) * 32);

        s16x8 af0[4], bq0[5], af1[4], bq1[5];
        auto rd = [&](s16x8* af, s16x8* bq, int kt) {
            const char* base = smem + (kt & 3) * K1_STAGE;
#pragma unroll
            for (int m = 0; m < 4; ++m) af[m] = *(const s16x8*)(base + a_off[m]);
#pragma unroll
            for (int n = 0; n < 5; ++n) {
                const char* p = base + b_off[n];
                bq[n] = cat8(tr_read(p), tr_read(p + 4 * 320));
            }
        };
        auto mm = [&](const s16x8* af, const s16x8* bq) {
#pragma unroll
            for (int n = 0; n < 5; ++n) {
#pragma unroll
                for (int m = 0; m < 4; ++m) {
                    if (IS_Q) acc4[m][n] = __builtin_amdgcn_mfma_f32_16x16x32_bf16(bq[n], af[m], acc4[m][n], 0, 0, 0);
                    else      acc4[m][n] = __builtin_amdgcn_mfma_f32_16x16x32_bf16(af[m], bq[n], acc4[m][n], 0, 0, 0);
                }
            }
        };
        BAR();
        rd(af0, bq0, 0);
#pragma unroll 1
        for (int kt = 0; kt < 10; kt += 2) {
            BAR();
            STAMP(0,1+kt);
            __builtin_amdgcn_s_waitcnt(0xC07F);
            __builtin_amdgcn_sched_barrier(0);
            rd(af1, bq1, kt + 1);
            __builtin_amdgcn_sched_barrier(0);
            mm(af0, bq0);
            __builtin_amdgcn_sched_barrier(0);
            BAR();
            STAMP(0,2+kt);
            __builtin_amdgcn_s_waitcnt(0xC07F);
            __builtin_amdgcn_sched_barrier(0);
            rd(af0, bq0, kt + 2);
            __builtin_amdgcn_sched_barrier(0);
            mm(af1, bq1);
            __builtin_amdgcn_sched_barrier(0);
        }
    }
    f32x4 acc[2][5];
    WAIT_L(0);
    BAR();
    if (wv < 8) {
        char* xb = smem + (wv * 10 * 64 + lane) * 16;
#pragma unroll
        for (int m = 0; m < 2; ++m)
#pragma unroll
            for (int n = 0; n < 5; ++n)
                *(f32x4*)(xb + (m * 5 + n) * 1024) = kh ? acc4[m][n] : acc4[2 + m][n];
    }
    __syncthreads();
    if (wv < 8) {
        const char* xb = smem + ((wv ^ 4) * 10 * 64 + lane) * 16;
#pragma unroll
        for (int m = 0; m < 2; ++m)
#pragma unroll
            for (int n = 0; n < 5; ++n)
                acc[m][n] = (kh ? acc4[2 + m][n] : acc4[m][n]) + *(const f32x4*)(xb + (m * 5 + n) * 1024);
    }
    STAMP(0,11);

    if (IS_Q) {
        if (wv >= 8) return;
#pragma unroll
        for (int m = 0; m < 2; ++m) {
            int s = row0 + wr * 32 + m * 16 + r16;
#pragma unroll
            for (int n = 0; n < 5; ++n) {
                u32x2 v;
                v[0] = pack2(phi(acc[m][n][0]), phi(acc[m][n][1]));
                v[1] = pack2(phi(acc[m][n][2]), phi(acc[m][n][3]));
                *(u32x2*)(pq + (size_t)s * D_ + cg * 160 + wc * 80 + n * 16 + 4 * g) = v;
            }
        }
    } else {
        s16x8 fr[5];
#pragma unroll
        for (int n = 0; n < 5; ++n) {
#pragma unroll
            for (int j = 0; j < 4; ++j) {
                float a0 = acc[0][n][j], a1 = acc[1][n][j];
                if (wc == 0) { a0 = phi(a0); a1 = phi(a1); }
                fr[n][j] = (short)f2bf(a0);
                fr[n][4 + j] = (short)f2bf(a1);
            }
        }
        WAIT_L(0);
        BAR();
        char* xch = smem + 96 * 1024;
        if (wc == 1 && wv < 8) {
#pragma unroll
            for (int n = 0; n < 5; ++n) *(s16x8*)(xch + ((wr * 5 + n) * 64 + lane) * 16) = fr[n];
        }
        __syncthreads();
        if (wc == 0 && wv < 8) {
            s16x8 vf[5];
#pragma unroll
            for (int n = 0; n < 5; ++n) vf[n] = *(const s16x8*)(xch + ((wr * 5 + n) * 64 + lane) * 16);
            s16x8 ones;
#pragma unroll
            for (int j = 0; j < 8; ++j) ones[j] = (short)0x3F80;
            float* red = (float*)smem + wr * PSZ;
            const f32x4 zero4 = (f32x4){0.f, 0.f, 0.f, 0.f};
            int pi = 0;
#pragma unroll
            for (int kt2 = 0; kt2 < 5; ++kt2) {
#pragma unroll
                for (int dv = -1; dv <= 1; ++dv) {
                    int vt = kt2 + dv;
                    if (vt < 0 || vt > 4) continue;
                    f32x4 dd = __builtin_amdgcn_mfma_f32_16x16x32_bf16(fr[kt2], vf[vt], zero4, 0, 0, 0);
#pragma unroll
                    for (int r = 0; r < 4; ++r) red[pi * 256 + (4 * g + r) * 16 + r16] = dd[r];
                    ++pi;
                }
                f32x4 zz = __builtin_amdgcn_mfma_f32_16x16x32_bf16(fr[kt2], ones, zero4, 0, 0, 0);
                if (r16 == 0) *(f32x4*)(red + 13 * 256 + kt2 * 16 + 4 * g) = zz;
            }
        }
        __syncthreads();
        const float* rb = (const float*)smem;
        float* dst = part + ((size_t)rt * 8 + (cg - 4)) * PSZ;
        for (int i = tid; i < PSZ; i += 640)
            dst[i] = (rb[i] + rb[PSZ + i]) + (rb[2 * PSZ + i] + rb[3 * PSZ + i]);
    }
    STAMP(0,12);
}

__global__ __launch_bounds__(640) void k1_kernel(const u16* __restrict__ x, const u16* __restrict__ w,
                                                 u16* __restrict__ pq, float* __restrict__ part,
                                                 unsigned* __restrict__ cnt) {
    __shared__ __attribute__((aligned(16))) char smem[K1_NST * K1_STAGE];
    int b = blockIdx.x;
    if (b == 0 && threadIdx.x < 8) cnt[threadIdx.x * 64] = 0u;
    int xcd = b & 7, idx = b >> 3;
    int rt = (xcd >> 1) * 4 + (idx & 3);
    int cg = (xcd & 1) * 6 + (idx >> 2);
    if (cg < 4) k1_body<true>(x, w, pq, part, smem, rt, cg);
    else        k1_body<false>(x, w, pq, part, smem, rt, cg);
}

constexpr int OST = 648;
constexpr int K2_O_BYTES = 64 * OST * 2;
constexpr int K2_CH_BYTES = 128 * 80 * 2;
typedef __attribute__((ext_vector_type(16))) float f32x16;
__device__ __forceinline__ int wo_col(int krow, int cc) {
    int t = (cc >> 1) + 5 - ((krow >> 3) & 1);
    t = t >= 5 ? t - 5 : t;
    return t * 16 + (cc & 1) * 8;
}

__global__ __launch_bounds__(640) void k2_kernel(const u16* __restrict__ pq, const float* __restrict__ part,
                                                 u16* kvfrag, unsigned* cnt,
                                                 const u16* __restrict__ wo, u16* __restrict__ out) {
    __shared__ __attribute__((aligned(16))) char smem[K2_O_BYTES + 3 * K2_CH_BYTES];
    const int tid = threadIdx.x, lane = tid & 63;
    const int wv = __builtin_amdgcn_readfirstlane(tid >> 6);
    const int g = lane >> 4, r16 = lane & 15;
    const int b = blockIdx.x;
    const int xcd = b & 7, idx = b >> 3;
    const int rt = xcd * 4 + (idx & 3), ct = idx >> 2;
    const int row0 = rt * 64, col0 = ct * 80;
    char* oimg = smem;
    char* wbuf = smem + K2_O_BYTES;

    if (wv >= 8) {
        const int ld = wv - 8;
        if (b < 32) { __syncthreads(); __syncthreads(); __syncthreads(); }
#pragma unroll
        for (int ch = 0; ch < 3; ++ch)
#pragma unroll
            for (int i = 0; i < 10; ++i) {
                int c = (ld * 10 + i) * 64 + lane;
                const u16* src = wo + (size_t)(ch * 128 + c / 10) * D_ + col0 + wo_col(c / 10, c % 10);
                __builtin_amdgcn_global_load_lds((const unsigned*)src,
                    (unsigned*)(wbuf + ch * K2_CH_BYTES + (ld * 10 + i) * 1024 + lane * 16), 16, 0, 0);
            }
        u32x4 hold[20];
#pragma unroll
        for (int i = 0; i < 20; ++i) {
            int c = i * 64 + lane;
            hold[i] = *(const u32x4*)(wo + (size_t)((3 + ld) * 128 + c / 10) * D_ + col0 + wo_col(c / 10, c % 10));
        }
        WAIT_V(0);
        BAR();
        BAR();
        BAR();
        if (ld == 0) {
#pragma unroll
            for (int i = 0; i < 20; ++i) *(u32x4*)(wbuf + i * 1024 + lane * 16) = hold[i];
        }
        WAIT_L(0);
        BAR();
        if (ld == 1) {
#pragma unroll
            for (int i = 0; i < 20; ++i) *(u32x4*)(wbuf + K2_CH_BYTES + i * 1024 + lane * 16) = hold[i];
        }
        WAIT_L(0);
        BAR();
        BAR();
        return;
    }

    STAMP(2,0);
    float psum = 0.f;
    if (b < 32 && tid < 420) {
        int grp = b >> 2, hh = b & 3;
        int pidx;
        if (tid < 400) {
            int d = hh * 20 + tid / 20, e = hh * 20 + tid % 20;
            int kt = d >> 4, vt = e >> 4;
            int pi = kt == 0 ? vt : (3 * kt - 1 + (vt - kt + 1));
            pidx = pi * 256 + (d & 15) * 16 + (e & 15);
        } else {
            pidx = 13 * 256 + hh * 20 + (tid - 400);
        }
        float v[NRT];
#pragma unroll
        for (int p = 0; p < NRT; ++p) v[p] = part[((size_t)p * 8 + grp) * PSZ + pidx];
#pragma unroll
        for (int p = 0; p < NRT; ++p) psum += v[p];
    }
    u32x4 pv[10];
#pragma unroll
    for (int j = 0; j < 10; ++j) {
        int c = j * 512 + tid;
        int row = c / 80, cc = c % 80;
        pv[j] = *(const u32x4*)(pq + (size_t)(row0 + row) * D_ + cc * 8);
    }

    if (b < 32) {
        u16* red = (u16*)wbuf;
        if (tid < 128) *(u32x4*)(red + tid * 8) = (u32x4){0u, 0u, 0u, 0u};
        __syncthreads();
        if (tid < 400) red[(tid % 20) * 32 + tid / 20] = f2bf(psum);
        else if (tid < 420) red[20 * 32 + (tid - 400)] = f2bf(psum);
        __syncthreads();
        if (tid < 128) {
            int ks = tid >> 6;
            u32x4 ov = *(const u32x4*)(red + (lane & 31) * 32 + 16 * ks + 8 * (lane >> 5));
            unsigned long long* dst = (unsigned long long*)(kvfrag + ((size_t)(b * 2 + ks) * 64 + lane) * 8);
            __hip_atomic_store(dst, (unsigned long long)ov[0] | ((unsigned long long)ov[1] << 32), __ATOMIC_RELAXED, __HIP_MEMORY_SCOPE_AGENT);
            __hip_atomic_store(dst + 1, (unsigned long long)ov[2] | ((unsigned long long)ov[3] << 32), __ATOMIC_RELAXED, __HIP_MEMORY_SCOPE_AGENT);
        }
        WAIT_V(0);
        __syncthreads();
        if (tid < 8) __hip_atomic_fetch_add(cnt + tid * 64, 1u, __ATOMIC_RELAXED, __HIP_MEMORY_SCOPE_AGENT);
        STAMP(1,0);
    }

#pragma unroll
    for (int j = 0; j < 10; ++j) {
        int c = j * 512 + tid;
        int row = c / 80, cc = c % 80;
        *(u32x4*)(oimg + row * (OST * 2) + cc * 16) = pv[j];
    }
    if (tid < 64) *(u32x4*)(oimg + tid * (OST * 2) + 1280) = (u32x4){0u, 0u, 0u, 0u};

    if (tid == 0) {
        unsigned spins = 0;
        while (__hip_atomic_load(cnt + (b & 7) * 64, __ATOMIC_RELAXED, __HIP_MEMORY_SCOPE_AGENT) < 32u) {
            __builtin_amdgcn_s_sleep(4);
            if (++spins > (1u << 24)) break;
        }
        __builtin_amdgcn_fence(__ATOMIC_ACQUIRE, "wavefront");
    }
    WAIT_L(0);
    BAR();
    STAMP(2,8);

    s16x8 a0[4], a1[4];
    {
        const int row = lane & 31;
#pragma unroll
        for (int hh = 0; hh < 4; ++hh) {
            int h = wv * 4 + hh;
            u32x4 v0 = (u32x4){0u, 0u, 0u, 0u}, v1 = (u32x4){0u, 0u, 0u, 0u};
            if (row <= 20) {
                const unsigned long long* s0 = (const unsigned long long*)(kvfrag + ((size_t)(h * 2 + 0) * 64 + lane) * 8);
                unsigned long long t0 = __hip_atomic_load(s0, __ATOMIC_RELAXED, __HIP_MEMORY_SCOPE_AGENT);
                unsigned long long t1 = __hip_atomic_load(s0 + 1, __ATOMIC_RELAXED, __HIP_MEMORY_SCOPE_AGENT);
                v0 = (u32x4){(unsigned)t0, (unsigned)(t0 >> 32), (unsigned)t1, (unsigned)(t1 >> 32)};
                if (lane < 32) {
                    const unsigned long long* s1 = (const unsigned long long*)(kvfrag + ((size_t)(h * 2 + 1) * 64 + lane) * 8);
                    unsigned long long t2 = __hip_atomic_load(s1, __ATOMIC_RELAXED, __HIP_MEMORY_SCOPE_AGENT);
                    unsigned long long t3 = __hip_atomic_load(s1 + 1, __ATOMIC_RELAXED, __HIP_MEMORY_SCOPE_AGENT);
                    v1 = (u32x4){(unsigned)t2, (unsigned)(t2 >> 32), (unsigned)t3, (unsigned)(t3 >> 32)};
                }
            }
            a0[hh] = __builtin_bit_cast(s16x8, v0);
            a1[hh] = __builtin_bit_cast(s16x8, v1);
        }
    }
    STAMP(2,9);
    {
        const int hl = lane >> 5;
        char* rbase = oimg + (lane & 31) * (OST * 2) + wv * 160 + hl * 16;
        char* wbase = oimg + (lane & 31) * (OST * 2) + wv * 160 + hl * 8;
        f32x16 zero16;
#pragma unroll
        for (int i = 0; i < 16; ++i) zero16[i] = 0.f;
#pragma unroll
        for (int hh = 0; hh < 4; ++hh) {
#pragma unroll
            for (int sh = 0; sh < 2; ++sh) {
                const char* rp = rbase + sh * 32 * (OST * 2) + hh * 40;
                u32x2 l0 = *(const u32x2*)(rp), l1 = *(const u32x2*)(rp + 8);
                u32x2 l2 = *(const u32x2*)(rp + 32), l3 = *(const u32x2*)(rp + 40);
                u32x4 b0 = (u32x4){l0[0], l0[1], l1[0], l1[1]};
                u32x4 b1 = (u32x4){l2[0], l2[1], l3[0], l3[1]};
                f32x16 dd = __builtin_amdgcn_mfma_f32_32x32x16_bf16(a0[hh], __builtin_bit_cast(s16x8, b0), zero16, 0, 0, 0);
                dd = __builtin_amdgcn_mfma_f32_32x32x16_bf16(a1[hh], __builtin_bit_cast(s16x8, b1), dd, 0, 0, 0);
                unsigned du = __float_as_uint(dd[8]);
                auto sw = __builtin_amdgcn_permlane32_swap(du, du, false, false);
                float den = __uint_as_float(sw[1]);
                den = rbf(rbf(den) + 1e-6f);
                float inv = __builtin_amdgcn_rcpf(den);
                char* wp = wbase + sh * 32 * (OST * 2) + hh * 40;
#pragma unroll
                for (int q = 0; q < 3; ++q) {
                    u32x2 v;
                    v[0] = pack2(rbf(dd[4 * q + 0]) * inv, rbf(dd[4 * q + 1]) * inv);
                    v[1] = pack2(rbf(dd[4 * q + 2]) * inv, rbf(dd[4 * q + 3]) * inv);
                    if (q < 2) *(u32x2*)(wp + q * 16) = v;
                    else if (hl == 0) *(u32x2*)(wp + 32) = v;
                }
            }
        }
    }
    STAMP(2,1);

    const int wr = wv & 3, wc = wv >> 2;
    const f32x4 zero4 = (f32x4){0.f, 0.f, 0.f, 0.f};
    f32x4 acc[3];
#pragma unroll
    for (int n = 0; n < 3; ++n) acc[n] = zero4;
    const int q4 = (lane & 15) >> 2, p4 = lane & 3;
    const int w_off0 = (8 * g + q4) * 160 + p4 * 8;
    int t_off[3];
#pragma unroll
    for (int n = 0; n < 3; ++n) { int u = wc * 3 + n + (g & 1); t_off[n] = (u >= 5 ? u - 5 : u) * 32; }
    const int orow = wr * 16 + r16;
    const char* obase = oimg + orow * (OST * 2) + g * 16;

    auto phase_b = [&](auto NT) {
        constexpr int nt = decltype(NT)::value;
#pragma unroll 1
        for (int ch = 0; ch < 5; ++ch) {
            WAIT_L(0);
            BAR();
            STAMP(2,2+ch);
            const char* wb = wbuf + (ch >= 3 ? ch - 3 : ch) * K2_CH_BYTES + w_off0;
            const char* ob = obase + ch * 256;
            s16x8 of[4], wf[4][nt];
#pragma unroll
            for (int ks = 0; ks < 4; ++ks) {
                of[ks] = *(const s16x8*)(ob + ks * 64);
#pragma unroll
                for (int n = 0; n < nt; ++n) {
                    const char* p = wb + (32 * ks) * 160 + t_off[n];
                    wf[ks][n] = cat8(tr_read(p), tr_read(p + 4 * 160));
                }
            }
#pragma unroll
            for (int ks = 0; ks < 4; ++ks)
#pragma unroll
                for (int n = 0; n < nt; ++n)
                    acc[n] = __builtin_amdgcn_mfma_f32_16x16x32_bf16(wf[ks][n], of[ks], acc[n], 0, 0, 0);
        }
    };
    if (wc == 0) phase_b(std::integral_constant<int, 3>{});
    else         phase_b(std::integral_constant<int, 2>{});
    STAMP(2,7);
    int s = row0 + orow;
#pragma unroll
    for (int n = 0; n < 3; ++n) {
        if (n == 2 && wc == 1) continue;
        u32x2 v;
        v[0] = pack2(acc[n][0], acc[n][1]);
        v[1] = pack2(acc[n][2], acc[n][3]);
        *(u32x2*)(out + (size_t)s * D_ + col0 + (wc * 3 + n) * 16 + 4 * g) = v;
    }
}

extern "C" void kernel_launch(void* const* d_in, const int* in_sizes, int n_in,
                              void* d_out, int out_size, void* d_ws, size_t ws_size,
                              hipStream_t stream) {
    const u16* x = (const u16*)d_in[0];
    const u16* wqkv = (const u16*)d_in[1];
    const u16* wo = (const u16*)d_in[2];
    u16* out = (u16*)d_out;
    char* ws = (char*)d_ws;
    u16* pq = (u16*)ws;
    float* part = (float*)(ws + 4u * 1024 * 1024);
    u16* kvfrag = (u16*)(ws + 7u * 1024 * 1024);
    unsigned* cnt = (unsigned*)(ws + 8u * 1024 * 1024);
    k1_kernel<<<192, 640, 0, stream>>>(x, wqkv, pq, part, cnt);
    k2_kernel<<<256, 640, 0, stream>>>(pq, part, kvfrag, cnt, wo, out);
}
```

```cpp
#include <hip/hip_runtime.h>
#include <hip/hip_bf16.h>
#include <stdint.h>
#include <type_traits>

typedef unsigned short u16;
typedef __attribute__((ext_vector_type(8))) short s16x8;
typedef __attribute__((ext_vector_type(4))) short s16x4;
typedef __attribute__((ext_vector_type(4))) float f32x4;
typedef __attribute__((ext_vector_type(2))) unsigned int u32x2;
typedef __attribute__((ext_vector_type(4))) unsigned int u32x4;

constexpr int S_ = 2048, D_ = 640, H_ = 32, DH_ = 20, N3_ = 1920;
constexpr int NRT = 16;
constexpr int PSZ = 13 * 256 + 80;

#define LDS_AS __attribute__((address_space(3)))

#define STAMP(k,i)

__device__ __forceinline__ float bf2f(u16 u) { return __uint_as_float(((unsigned)u) << 16); }
__device__ __forceinline__ u16 f2bf(float f) { __bf16 b = (__bf16)f; return __builtin_bit_cast(u16, b); }
__device__ __forceinline__ float rbf(float f) { return bf2f(f2bf(f)); }
__device__ __forceinline__ unsigned pack2(float a, float b) { return (unsigned)f2bf(a) | ((unsigned)f2bf(b) << 16); }
__device__ __forceinline__ float phi(float acc) {
    float t = rbf(acc);
    float p = t > 0.f ? t + 1.0f : __expf(t);
    return rbf(rbf(p) + 1e-6f);
}

#define WAIT_V(n) asm volatile("s_waitcnt vmcnt(" #n ")" ::: "memory")
#define WAIT_L(n) asm volatile("s_waitcnt lgkmcnt(" #n ")" ::: "memory")
#define BAR() __builtin_amdgcn_s_barrier()

__device__ __forceinline__ s16x4 tr_read(const char* lds_ptr) {
    return __builtin_amdgcn_ds_read_tr16_b64_v4i16((LDS_AS s16x4*)lds_ptr);
}
__device__ __forceinline__ s16x8 cat8(s16x4 a, s16x4 b) {
    s16x8 r; r[0]=a[0]; r[1]=a[1]; r[2]=a[2]; r[3]=a[3]; r[4]=b[0]; r[5]=b[1]; r[6]=b[2]; r[7]=b[3]; return r;
}

constexpr int K1_A_BYTES = 128 * 64 * 2;
constexpr int K1_B_BYTES = 64 * 160 * 2;
constexpr int K1_STAGE = K1_A_BYTES + K1_B_BYTES;
constexpr int K1_NST = 4;

template <bool IS_Q>
__device__ __forceinline__ void k1_body(const u16* __restrict__ x, const u16* __restrict__ w,
                                        u16* __restrict__ pq, float* __restrict__ part,
                                        char* smem, int rt, int cg) {
    const int tid = threadIdx.x, lane = tid & 63;
    const int wv = __builtin_amdgcn_readfirstlane(tid >> 6);
    const int wr = wv & 3, wc = (wv >> 2) & 1;
    const int g = lane >> 4, r16 = lane & 15;
    const int row0 = rt * 128;

    f32x4 acc[2][5];
#pragma unroll
    for (int m = 0; m < 2; ++m)
#pragma unroll
        for (int n = 0; n < 5; ++n) acc[m][n] = (f32x4){0.f, 0.f, 0.f, 0.f};

    STAMP(0,0);
    if (wv >= 8) {
        const int ld = wv - 8;
        int a_src[8], b_src[10];
#pragma unroll
        for (int i = 0; i < 8; ++i) {
            int row = (ld * 8 + i) * 8 + (lane >> 3), slot = lane & 7;
            a_src[i] = (row0 + row) * D_ + ((slot ^ (row & 7)) * 8);
        }
#pragma unroll
        for (int i = 0; i < 10; ++i) {
            int c = (ld * 10 + i) * 64 + lane;
            int krow = c / 20, cc = c % 20;
            int sc = cc ^ (((krow >> 3) & 1) << 1);
            int col = sc * 8;
            int gcol;
            if (IS_Q) gcol = cg * 160 + col;
            else gcol = (col < 80) ? (640 + (cg - 4) * 80 + col) : (1280 + (cg - 4) * 80 + (col - 80));
            b_src[i] = krow * N3_ + gcol;
        }
        auto stage = [&](int st, int kt) {
            char* a_dst = smem + st * K1_STAGE + ld * 8192 + lane * 16;
            char* b_dst = smem + st * K1_STAGE + K1_A_BYTES + ld * 10240 + lane * 16;
#pragma unroll
            for (int i = 0; i < 8; ++i)
                __builtin_amdgcn_global_load_lds((const unsigned*)(x + a_src[i] + kt * 64), (unsigned*)(a_dst + i * 1024), 16, 0, 0);
#pragma unroll
            for (int i = 0; i < 10; ++i)
                __builtin_amdgcn_global_load_lds((const unsigned*)(w + b_src[i] + kt * 64 * N3_), (unsigned*)(b_dst + i * 1024), 16, 0, 0);
        };
        stage(0, 0); stage(1, 1); stage(2, 2);
        WAIT_V(36);
        BAR();
#pragma unroll 1
        for (int kt = 0; kt < 10; ++kt) {
            if (kt < 7) WAIT_V(18); else WAIT_V(0);
            BAR();
            if (kt + 3 < 10) stage((kt + 3) & 3, kt + 3);
        }
    } else {
        int a_off[2][2];
#pragma unroll
        for (int m = 0; m < 2; ++m)
#pragma unroll
            for (int ks = 0; ks < 2; ++ks) {
                int row = wr * 32 + m * 16 + r16;
                a_off[m][ks] = row * 128 + (((4 * ks + g) ^ (row & 7)) * 16);
            }
        const int q4 = (lane & 15) >> 2, p4 = lane & 3;
        int b_off[5];
#pragma unroll
        for (int n = 0; n < 5; ++n) b_off[n] = (8 * g + q4) * 320 + p4 * 8 + (((wc * 5 + n) ^ (g & 1)) * 32);

        s16x8 af0[2], bq0[5], af1[2], bq1[5];
        auto rd = [&](s16x8* af, s16x8* bq, int kt, int ks) {
            const char* a_base = smem + (kt & 3) * K1_STAGE;
            const char* b_base = a_base + K1_A_BYTES;
#pragma unroll
            for (int m = 0; m < 2; ++m) af[m] = *(const s16x8*)(a_base + a_off[m][ks]);
#pragma unroll
            for (int n = 0; n < 5; ++n) {
                const char* p = b_base + b_off[n] + (32 * ks) * 320;
                bq[n] = cat8(tr_read(p), tr_read(p + 4 * 320));
            }
        };
        auto mm = [&](const s16x8* af, const s16x8* bq) {
#pragma unroll
            for (int n = 0; n < 5; ++n) {
#pragma unroll
                for (int m = 0; m < 2; ++m) {
                    if (IS_Q) acc[m][n] = __builtin_amdgcn_mfma_f32_16x16x32_bf16(bq[n], af[m], acc[m][n], 0, 0, 0);
                    else      acc[m][n] = __builtin_amdgcn_mfma_f32_16x16x32_bf16(af[m], bq[n], acc[m][n], 0, 0, 0);
                }
            }
        };
        BAR();
        rd(af0, bq0, 0, 0);
#pragma unroll 1
        for (int kt = 0; kt < 10; ++kt) {
            BAR();
            STAMP(0,1+kt);
            __builtin_amdgcn_s_waitcnt(0xC07F);
            __builtin_amdgcn_sched_barrier(0);
            rd(af1, bq1, kt, 1);
            __builtin_amdgcn_sched_barrier(0);
            mm(af0, bq0);
            __builtin_amdgcn_sched_barrier(0);
            rd(af0, bq0, kt + 1, 0);
            __builtin_amdgcn_sched_barrier(0);
            mm(af1, bq1);
            __builtin_amdgcn_sched_barrier(0);
        }
    }
    STAMP(0,11);

    if (IS_Q) {
        if (wv >= 8) return;
#pragma unroll
        for (int m = 0; m < 2; ++m) {
            int s = row0 + wr * 32 + m * 16 + r16;
#pragma unroll
            for (int n = 0; n < 5; ++n) {
                u32x2 v;
                v[0] = pack2(phi(acc[m][n][0]), phi(acc[m][n][1]));
                v[1] = pack2(phi(acc[m][n][2]), phi(acc[m][n][3]));
                *(u32x2*)(pq + (size_t)s * D_ + cg * 160 + wc * 80 + n * 16 + 4 * g) = v;
            }
        }
    } else {
        s16x8 fr[5];
#pragma unroll
        for (int n = 0; n < 5; ++n) {
#pragma unroll
            for (int j = 0; j < 4; ++j) {
                float a0 = acc[0][n][j], a1 = acc[1][n][j];
                if (wc == 0) { a0 = phi(a0); a1 = phi(a1); }
                fr[n][j] = (short)f2bf(a0);
                fr[n][4 + j] = (short)f2bf(a1);
            }
        }
        WAIT_L(0);
        BAR();
        char* xch = smem + 64 * 1024;
        if (wc == 1 && wv < 8) {
#pragma unroll
            for (int n = 0; n < 5; ++n) *(s16x8*)(xch + ((wr * 5 + n) * 64 + lane) * 16) = fr[n];
        }
        __syncthreads();
        if (wv < 4) {
            s16x8 vf[5];
#pragma unroll
            for (int n = 0; n < 5; ++n) vf[n] = *(const s16x8*)(xch + ((wr * 5 + n) * 64 + lane) * 16);
            s16x8 ones;
#pragma unroll
            for (int j = 0; j < 8; ++j) ones[j] = (short)0x3F80;
            float* red = (float*)smem + wr * PSZ;
            const f32x4 zero4 = (f32x4){0.f, 0.f, 0.f, 0.f};
            int pi = 0;
#pragma unroll
            for (int kt2 = 0; kt2 < 5; ++kt2) {
#pragma unroll
                for (int dv = -1; dv <= 1; ++dv) {
                    int vt = kt2 + dv;
                    if (vt < 0 || vt > 4) continue;
                    f32x4 dd = __builtin_amdgcn_mfma_f32_16x16x32_bf16(vf[vt], fr[kt2], zero4, 0, 0, 0);
                    *(f32x4*)(red + pi * 256 + r16 * 16 + 4 * g) = dd;
                    ++pi;
                }
                f32x4 zz = __builtin_amdgcn_mfma_f32_16x16x32_bf16(fr[kt2], ones, zero4, 0, 0, 0);
                if (r16 == 0) *(f32x4*)(red + 13 * 256 + kt2 * 16 + 4 * g) = zz;
            }
        }
        __syncthreads();
        const f32x4* rb = (const f32x4*)smem;
        f32x4* dst = (f32x4*)(part + ((size_t)rt * 8 + (cg - 4)) * PSZ);
        for (int i = tid; i < PSZ / 4; i += 640)
            dst[i] = (rb[i] + rb[PSZ / 4 + i]) + (rb[2 * (PSZ / 4) + i] + rb[3 * (PSZ / 4) + i]);
    }
    STAMP(0,12);
}

__global__ __launch_bounds__(640) void k1_kernel(const u16* __restrict__ x, const u16* __restrict__ w,
                                                 u16* __restrict__ pq, float* __restrict__ part,
                                                 unsigned* __restrict__ cnt) {
    __shared__ __attribute__((aligned(16))) char smem[K1_NST * K1_STAGE];
    int b = blockIdx.x;
    if (b == 0 && threadIdx.x < 8) cnt[threadIdx.x * 64] = 0u;
    int xcd = b & 7, idx = b >> 3;
    int rt = (xcd >> 1) * 4 + (idx & 3);
    int cg = (xcd & 1) * 6 + (idx >> 2);
    if (cg < 4) k1_body<true>(x, w, pq, part, smem, rt, cg);
    else        k1_body<false>(x, w, pq, part, smem, rt, cg);
}

constexpr int OST = 648;
constexpr int K2_O_BYTES = 64 * OST * 2;
constexpr int K2_CH_BYTES = 128 * 80 * 2;
typedef __attribute__((ext_vector_type(16))) float f32x16;
__device__ __forceinline__ int wo_col(int krow, int cc) {
    int t = (cc >> 1) + 5 - ((krow >> 3) & 1);
    t = t >= 5 ? t - 5 : t;
    return t * 16 + (cc & 1) * 8;
}

__global__ __launch_bounds__(640) void k2_kernel(const u16* __restrict__ pq, const float* __restrict__ part,
                                                 u16* kvfrag, unsigned* cnt,
                                                 const u16* __restrict__ wo, u16* __restrict__ out) {
    __shared__ __attribute__((aligned(16))) char smem[K2_O_BYTES + 3 * K2_CH_BYTES];
    const int tid = threadIdx.x, lane = tid & 63;
    const int wv = __builtin_amdgcn_readfirstlane(tid >> 6);
    const int g = lane >> 4, r16 = lane & 15;
    const int b = blockIdx.x;
    const int xcd = b & 7, idx = b >> 3;
    const int rt = xcd * 4 + (idx & 3), ct = idx >> 2;
    const int row0 = rt * 64, col0 = ct * 80;
    char* oimg = smem;
    char* wbuf = smem + K2_O_BYTES;

    if (wv >= 8) {
        const int ld = wv - 8;
        if (b < 32) { __syncthreads(); __syncthreads(); __syncthreads(); }
#pragma unroll
        for (int ch = 0; ch < 3; ++ch)
#pragma unroll
            for (int i = 0; i < 10; ++i) {
                int c = (ld * 10 + i) * 64 + lane;
                const u16* src = wo + (size_t)(ch * 128 + c / 10) * D_ + col0 + wo_col(c / 10, c % 10);
                __builtin_amdgcn_global_load_lds((const unsigned*)src,
                    (unsigned*)(wbuf + ch * K2_CH_BYTES + (ld * 10 + i) * 1024 + lane * 16), 16, 0, 0);
            }
        u32x4 hold[20];
#pragma unroll
        for (int i = 0; i < 20; ++i) {
            int c = i * 64 + lane;
            hold[i] = *(const u32x4*)(wo + (size_t)((3 + ld) * 128 + c / 10) * D_ + col0 + wo_col(c / 10, c % 10));
        }
        WAIT_V(0);
        BAR();
        BAR();
        BAR();
        if (ld == 0) {
#pragma unroll
            for (int i = 0; i < 20; ++i) *(u32x4*)(wbuf + i * 1024 + lane * 16) = hold[i];
        }
        WAIT_L(0);
        BAR();
        if (ld == 1) {
#pragma unroll
            for (int i = 0; i < 20; ++i) *(u32x4*)(wbuf + K2_CH_BYTES + i * 1024 + lane * 16) = hold[i];
        }
        WAIT_L(0);
        BAR();
        BAR();
        return;
    }

    STAMP(2,0);
    float psum = 0.f;
    if (b < 32 && tid < 420) {
        int grp = b >> 2, hh = b & 3;
        int pidx;
        if (tid < 400) {
            int d = hh * 20 + tid / 20, e = hh * 20 + tid % 20;
            int kt = d >> 4, vt = e >> 4;
            int pi = kt == 0 ? vt : (3 * kt - 1 + (vt - kt + 1));
            pidx = pi * 256 + (d & 15) * 16 + (e & 15);
        } else {
            pidx = 13 * 256 + hh * 20 + (tid - 400);
        }
        float v[NRT];
#pragma unroll
        for (int p = 0; p < NRT; ++p) v[p] = part[((size_t)p * 8 + grp) * PSZ + pidx];
#pragma unroll
        for (int p = 0; p < NRT; ++p) psum += v[p];
    }
    u32x4 pv[10];
#pragma unroll
    for (int j = 0; j < 10; ++j) {
        int c = j * 512 + tid;
        int row = c / 80, cc = c % 80;
        pv[j] = *(const u32x4*)(pq + (size_t)(row0 + row) * D_ + cc * 8);
    }

    if (b < 32) {
        u16* red = (u16*)wbuf;
        if (tid < 128) *(u32x4*)(red + tid * 8) = (u32x4){0u, 0u, 0u, 0u};
        __syncthreads();
        if (tid < 400) red[(tid % 20) * 32 + tid / 20] = f2bf(psum);
        else if (tid < 420) red[20 * 32 + (tid - 400)] = f2bf(psum);
        __syncthreads();
        if (tid < 128) {
            int ks = tid >> 6;
            u32x4 ov = *(const u32x4*)(red + (lane & 31) * 32 + 16 * ks + 8 * (lane >> 5));
            unsigned long long* dst = (unsigned long long*)(kvfrag + ((size_t)(b * 2 + ks) * 64 + lane) * 8);
            __hip_atomic_store(dst, (unsigned long long)ov[0] | ((unsigned long long)ov[1] << 32), __ATOMIC_RELAXED, __HIP_MEMORY_SCOPE_AGENT);
            __hip_atomic_store(dst + 1, (unsigned long long)ov[2] | ((unsigned long long)ov[3] << 32), __ATOMIC_RELAXED, __HIP_MEMORY_SCOPE_AGENT);
        }
        WAIT_V(0);
        __syncthreads();
        if (tid < 8) __hip_atomic_fetch_add(cnt + tid * 64, 1u, __ATOMIC_RELAXED, __HIP_MEMORY_SCOPE_AGENT);
        STAMP(1,0);
    }

#pragma unroll
    for (int j = 0; j < 10; ++j) {
        int c = j * 512 + tid;
        int row = c / 80, cc = c % 80;
        *(u32x4*)(oimg + row * (OST * 2) + cc * 16) = pv[j];
    }
    if (tid < 64) *(u32x4*)(oimg + tid * (OST * 2) + 1280) = (u32x4){0u, 0u, 0u, 0u};

    if (tid == 0) {
        unsigned spins = 0;
        while (__hip_atomic_load(cnt + (b & 7) * 64, __ATOMIC_RELAXED, __HIP_MEMORY_SCOPE_AGENT) < 32u) {
            __builtin_amdgcn_s_sleep(4);
            if (++spins > (1u << 24)) break;
        }
        __builtin_amdgcn_fence(__ATOMIC_ACQUIRE, "wavefront");
    }
    WAIT_L(0);
    BAR();
    STAMP(2,8);

    s16x8 a0[4], a1[4];
    {
        const int row = lane & 31;
#pragma unroll
        for (int hh = 0; hh < 4; ++hh) {
            int h = wv * 4 + hh;
            u32x4 v0 = (u32x4){0u, 0u, 0u, 0u}, v1 = (u32x4){0u, 0u, 0u, 0u};
            if (row <= 20) {
                const unsigned long long* s0 = (const unsigned long long*)(kvfrag + ((size_t)(h * 2 + 0) * 64 + lane) * 8);
                unsigned long long t0 = __hip_atomic_load(s0, __ATOMIC_RELAXED, __HIP_MEMORY_SCOPE_AGENT);
                unsigned long long t1 = __hip_atomic_load(s0 + 1, __ATOMIC_RELAXED, __HIP_MEMORY_SCOPE_AGENT);
                v0 = (u32x4){(unsigned)t0, (unsigned)(t0 >> 32), (unsigned)t1, (unsigned)(t1 >> 32)};
                if (lane < 32) {
                    const unsigned long long* s1 = (const unsigned long long*)(kvfrag + ((size_t)(h * 2 + 1) * 64 + lane) * 8);
                    unsigned long long t2 = __hip_atomic_load(s1, __ATOMIC_RELAXED, __HIP_MEMORY_SCOPE_AGENT);
                    unsigned long long t3 = __hip_atomic_load(s1 + 1, __ATOMIC_RELAXED, __HIP_MEMORY_SCOPE_AGENT);
                    v1 = (u32x4){(unsigned)t2, (unsigned)(t2 >> 32), (unsigned)t3, (unsigned)(t3 >> 32)};
                }
            }
            a0[hh] = __builtin_bit_cast(s16x8, v0);
            a1[hh] = __builtin_bit_cast(s16x8, v1);
        }
    }
    STAMP(2,9);
    {
        const int hl = lane >> 5;
        char* rbase = oimg + (lane & 31) * (OST * 2) + wv * 160 + hl * 16;
        char* wbase = oimg + (lane & 31) * (OST * 2) + wv * 160 + hl * 8;
        f32x16 zero16;
#pragma unroll
        for (int i = 0; i < 16; ++i) zero16[i] = 0.f;
#pragma unroll
        for (int hh = 0; hh < 4; ++hh) {
#pragma unroll
            for (int sh = 0; sh < 2; ++sh) {
                const char* rp = rbase + sh * 32 * (OST * 2) + hh * 40;
                u32x2 l0 = *(const u32x2*)(rp), l1 = *(const u32x2*)(rp + 8);
                u32x2 l2 = *(const u32x2*)(rp + 32), l3 = *(const u32x2*)(rp + 40);
                u32x4 b0 = (u32x4){l0[0], l0[1], l1[0], l1[1]};
                u32x4 b1 = (u32x4){l2[0], l2[1], l3[0], l3[1]};
                f32x16 dd = __builtin_amdgcn_mfma_f32_32x32x16_bf16(a0[hh], __builtin_bit_cast(s16x8, b0), zero16, 0, 0, 0);
                dd = __builtin_amdgcn_mfma_f32_32x32x16_bf16(a1[hh], __builtin_bit_cast(s16x8, b1), dd, 0, 0, 0);
                unsigned du = __float_as_uint(dd[8]);
                auto sw = __builtin_amdgcn_permlane32_swap(du, du, false, false);
                float den = __uint_as_float(sw[1]);
                den = rbf(rbf(den) + 1e-6f);
                float inv = __builtin_amdgcn_rcpf(den);
                char* wp = wbase + sh * 32 * (OST * 2) + hh * 40;
#pragma unroll
                for (int q = 0; q < 3; ++q) {
                    u32x2 v;
                    v[0] = pack2(rbf(dd[4 * q + 0]) * inv, rbf(dd[4 * q + 1]) * inv);
                    v[1] = pack2(rbf(dd[4 * q + 2]) * inv, rbf(dd[4 * q + 3]) * inv);
                    if (q < 2) *(u32x2*)(wp + q * 16) = v;
                    else if (hl == 0) *(u32x2*)(wp + 32) = v;
                }
            }
        }
    }
    STAMP(2,1);

    const int wr = wv & 3, wc = wv >> 2;
    const f32x4 zero4 = (f32x4){0.f, 0.f, 0.f, 0.f};
    f32x4 acc[3];
#pragma unroll
    for (int n = 0; n < 3; ++n) acc[n] = zero4;
    const int q4 = (lane & 15) >> 2, p4 = lane & 3;
    const int w_off0 = (8 * g + q4) * 160 + p4 * 8;
    int t_off[3];
#pragma unroll
    for (int n = 0; n < 3; ++n) { int u = wc * 3 + n + (g & 1); t_off[n] = (u >= 5 ? u - 5 : u) * 32; }
    const int orow = wr * 16 + r16;
    const char* obase = oimg + orow * (OST * 2) + g * 16;

    auto phase_b = [&](auto NT) {
        constexpr int nt = decltype(NT)::value;
#pragma unroll 1
        for (int ch = 0; ch < 5; ++ch) {
            WAIT_L(0);
            BAR();
            STAMP(2,2+ch);
            const char* wb = wbuf + (ch >= 3 ? ch - 3 : ch) * K2_CH_BYTES + w_off0;
            const char* ob = obase + ch * 256;
            s16x8 of[4], wf[4][nt];
#pragma unroll
            for (int ks = 0; ks < 4; ++ks) {
                of[ks] = *(const s16x8*)(ob + ks * 64);
#pragma unroll
                for (int n = 0; n < nt; ++n) {
                    const char* p = wb + (32 * ks) * 160 + t_off[n];
                    wf[ks][n] = cat8(tr_read(p), tr_read(p + 4 * 160));
                }
            }
#pragma unroll
            for (int ks = 0; ks < 4; ++ks)
#pragma unroll
                for (int n = 0; n < nt; ++n)
                    acc[n] = __builtin_amdgcn_mfma_f32_16x16x32_bf16(wf[ks][n], of[ks], acc[n], 0, 0, 0);
        }
    };
    if (wc == 0) phase_b(std::integral_constant<int, 3>{});
    else         phase_b(std::integral_constant<int, 2>{});
    STAMP(2,7);
    int s = row0 + orow;
#pragma unroll
    for (int n = 0; n < 3; ++n) {
        if (n == 2 && wc == 1) continue;
        u32x2 v;
        v[0] = pack2(acc[n][0], acc[n][1]);
        v[1] = pack2(acc[n][2], acc[n][3]);
        *(u32x2*)(out + (size_t)s * D_ + col0 + (wc * 3 + n) * 16 + 4 * g) = v;
    }
}

extern "C" void kernel_launch(void* const* d_in, const int* in_sizes, int n_in,
                              void* d_out, int out_size, void* d_ws, size_t ws_size,
                              hipStream_t stream) {
    const u16* x = (const u16*)d_in[0];
    const u16* wqkv = (const u16*)d_in[1];
    const u16* wo = (const u16*)d_in[2];
    u16* out = (u16*)d_out;
    char* ws = (char*)d_ws;
    u16* pq = (u16*)ws;
    float* part = (float*)(ws + 4u * 1024 * 1024);
    u16* kvfrag = (u16*)(ws + 7u * 1024 * 1024);
    unsigned* cnt = (unsigned*)(ws + 8u * 1024 * 1024);
    k1_kernel<<<192, 640, 0, stream>>>(x, wqkv, pq, part, cnt);
    k2_kernel<<<256, 640, 0, stream>>>(pq, part, kvfrag, cnt, wo, out);
}
```

```cpp
#include <hip/hip_runtime.h>
#include <hip/hip_bf16.h>
#include <stdint.h>
#include <type_traits>

typedef unsigned short u16;
typedef __attribute__((ext_vector_type(8))) short s16x8;
typedef __attribute__((ext_vector_type(4))) short s16x4;
typedef __attribute__((ext_vector_type(4))) float f32x4;
typedef __attribute__((ext_vector_type(2))) unsigned int u32x2;
typedef __attribute__((ext_vector_type(4))) unsigned int u32x4;

constexpr int S_ = 2048, D_ = 640, H_ = 32, DH_ = 20, N3_ = 1920;
constexpr int NRT = 16;
constexpr int PSZ = 13 * 256 + 80;

#define LDS_AS __attribute__((address_space(3)))

#define STAMP(k,i)

__device__ __forceinline__ float bf2f(u16 u) { return __uint_as_float(((unsigned)u) << 16); }
__device__ __forceinline__ u16 f2bf(float f) { __bf16 b = (__bf16)f; return __builtin_bit_cast(u16, b); }
__device__ __forceinline__ float rbf(float f) { return bf2f(f2bf(f)); }
__device__ __forceinline__ unsigned pack2(float a, float b) { return (unsigned)f2bf(a) | ((unsigned)f2bf(b) << 16); }
__device__ __forceinline__ float phi(float acc) {
    float t = rbf(acc);
    float p = t > 0.f ? t + 1.0f : __expf(t);
    return rbf(rbf(p) + 1e-6f);
}

#define WAIT_V(n) asm volatile("s_waitcnt vmcnt(" #n ")" ::: "memory")
#define WAIT_L(n) asm volatile("s_waitcnt lgkmcnt(" #n ")" ::: "memory")
#define BAR() __builtin_amdgcn_s_barrier()

__device__ __forceinline__ s16x4 tr_read(const char* lds_ptr) {
    return __builtin_amdgcn_ds_read_tr16_b64_v4i16((LDS_AS s16x4*)lds_ptr);
}
__device__ __forceinline__ s16x8 cat8(s16x4 a, s16x4 b) {
    s16x8 r; r[0]=a[0]; r[1]=a[1]; r[2]=a[2]; r[3]=a[3]; r[4]=b[0]; r[5]=b[1]; r[6]=b[2]; r[7]=b[3]; return r;
}

constexpr int K1_A_BYTES = 128 * 64 * 2;
constexpr int K1_B_BYTES = 64 * 160 * 2;
constexpr int K1_STAGE = K1_A_BYTES + K1_B_BYTES;
constexpr int K1_NST = 4;

template <bool IS_Q>
__device__ __forceinline__ void k1_body(const u16* __restrict__ x, const u16* __restrict__ w,
                                        u16* __restrict__ pq, float* __restrict__ part,
                                        char* smem, int rt, int cg) {
    const int tid = threadIdx.x, lane = tid & 63;
    const int wv = __builtin_amdgcn_readfirstlane(tid >> 6);
    const int wr = wv & 3, wc = (wv >> 2) & 1;
    const int g = lane >> 4, r16 = lane & 15;
    const int row0 = rt * 128;

    f32x4 acc[2][5];
#pragma unroll
    for (int m = 0; m < 2; ++m)
#pragma unroll
        for (int n = 0; n < 5; ++n) acc[m][n] = (f32x4){0.f, 0.f, 0.f, 0.f};

    STAMP(0,0);
    if (wv >= 8) {
        const int ld = wv - 8;
        int a_src[8], b_src[10];
#pragma unroll
        for (int i = 0; i < 8; ++i) {
            int row = (ld * 8 + i) * 8 + (lane >> 3), slot = lane & 7;
            a_src[i] = (row0 + row) * D_ + ((slot ^ (row & 7)) * 8);
        }
#pragma unroll
        for (int i = 0; i < 10; ++i) {
            int c = (ld * 10 + i) * 64 + lane;
            int krow = c / 20, cc = c % 20;
            int sc = cc ^ (((krow >> 3) & 1) << 1);
            int col = sc * 8;
            int gcol;
            if (IS_Q) gcol = cg * 160 + col;
            else gcol = (col < 80) ? (640 + (cg - 4) * 80 + col) : (1280 + (cg - 4) * 80 + (col - 80));
            b_src[i] = krow * N3_ + gcol;
        }
        auto stage = [&](int st, int kt) {
            char* a_dst = smem + st * K1_STAGE + ld * 8192 + lane * 16;
            char* b_dst = smem + st * K1_STAGE + K1_A_BYTES + ld * 10240 + lane * 16;
#pragma unroll
            for (int i = 0; i < 8; ++i)
                __builtin_amdgcn_global_load_lds((const unsigned*)(x + a_src[i] + kt * 64), (unsigned*)(a_dst + i * 1024), 16, 0, 0);
#pragma unroll
            for (int i = 0; i < 10; ++i)
                __builtin_amdgcn_global_load_lds((const unsigned*)(w + b_src[i] + kt * 64 * N3_), (unsigned*)(b_dst + i * 1024), 16, 0, 0);
        };
        stage(0, 0); stage(1, 1); stage(2, 2);
        WAIT_V(36);
        BAR();
#pragma unroll 1
        for (int kt = 0; kt < 10; ++kt) {
            if (kt < 7) WAIT_V(18); else WAIT_V(0);
            BAR();
            if (kt + 3 < 10) stage((kt + 3) & 3, kt + 3);
        }
    } else {
        int a_off[2][2];
#pragma unroll
        for (int m = 0; m < 2; ++m)
#pragma unroll
            for (int ks = 0; ks < 2; ++ks) {
                int row = wr * 32 + m * 16 + r16;
                a_off[m][ks] = row * 128 + (((4 * ks + g) ^ (row & 7)) * 16);
            }
        const int q4 = (lane & 15) >> 2, p4 = lane & 3;
        int b_off[5];
#pragma unroll
        for (int n = 0; n < 5; ++n) {
            int tl = IS_Q ? wc * 5 + n : (wc == 0 ? (n < 3 ? n : n + 2) : (n < 2 ? n + 3 : n + 5));
            b_off[n] = (8 * g + q4) * 320 + p4 * 8 + ((tl ^ (g & 1)) * 32);
        }

        s16x8 af0[2], bq0[5], af1[2], bq1[5];
        auto rd = [&](s16x8* af, s16x8* bq, int kt, int ks) {
            const char* a_base = smem + (kt & 3) * K1_STAGE;
            const char* b_base = a_base + K1_A_BYTES;
#pragma unroll
            for (int m = 0; m < 2; ++m) af[m] = *(const s16x8*)(a_base + a_off[m][ks]);
#pragma unroll
            for (int n = 0; n < 5; ++n) {
                const char* p = b_base + b_off[n] + (32 * ks) * 320;
                bq[n] = cat8(tr_read(p), tr_read(p + 4 * 320));
            }
        };
        auto mm = [&](const s16x8* af, const s16x8* bq) {
#pragma unroll
            for (int n = 0; n < 5; ++n) {
#pragma unroll
                for (int m = 0; m < 2; ++m) {
                    if (IS_Q) acc[m][n] = __builtin_amdgcn_mfma_f32_16x16x32_bf16(bq[n], af[m], acc[m][n], 0, 0, 0);
                    else      acc[m][n] = __builtin_amdgcn_mfma_f32_16x16x32_bf16(af[m], bq[n], acc[m][n], 0, 0, 0);
                }
            }
        };
        BAR();
        rd(af0, bq0, 0, 0);
#pragma unroll 1
        for (int kt = 0; kt < 10; ++kt) {
            BAR();
            STAMP(0,1+kt);
            __builtin_amdgcn_s_waitcnt(0xC07F);
            __builtin_amdgcn_sched_barrier(0);
            rd(af1, bq1, kt, 1);
            __builtin_amdgcn_sched_barrier(0);
            mm(af0, bq0);
            __builtin_amdgcn_sched_barrier(0);
            rd(af0, bq0, kt + 1, 0);
            __builtin_amdgcn_sched_barrier(0);
            mm(af1, bq1);
            __builtin_amdgcn_sched_barrier(0);
        }
    }
    STAMP(0,11);

    if (IS_Q) {
        if (wv >= 8) return;
#pragma unroll
        for (int m = 0; m < 2; ++m) {
            int s = row0 + wr * 32 + m * 16 + r16;
#pragma unroll
            for (int n = 0; n < 5; ++n) {
                u32x2 v;
                v[0] = pack2(phi(acc[m][n][0]), phi(acc[m][n][1]));
                v[1] = pack2(phi(acc[m][n][2]), phi(acc[m][n][3]));
                *(u32x2*)(pq + (size_t)s * D_ + cg * 160 + wc * 80 + n * 16 + 4 * g) = v;
            }
        }
    } else {
        WAIT_L(0);
        BAR();
        char* xch = smem + 64 * 1024;
        if (wv < 8) {
            const int nk = wc == 0 ? 3 : 2;
#pragma unroll
            for (int n = 0; n < 5; ++n) {
                s16x8 fr;
#pragma unroll
                for (int j = 0; j < 4; ++j) {
                    float a0 = acc[0][n][j], a1 = acc[1][n][j];
                    if (n < 2 || (n == 2 && nk == 3)) { a0 = phi(a0); a1 = phi(a1); }
                    fr[j] = (short)f2bf(a0);
                    fr[4 + j] = (short)f2bf(a1);
                }
                int tl = wc == 0 ? (n < 3 ? n : n + 2) : (n < 2 ? n + 3 : n + 5);
                *(s16x8*)(xch + ((wr * 10 + tl) * 64 + lane) * 16) = fr;
            }
        }
        __syncthreads();
        if (wv < 8) {
            s16x8 ones;
#pragma unroll
            for (int j = 0; j < 8; ++j) ones[j] = (short)0x3F80;
            float* red = (float*)smem + wr * PSZ;
            const f32x4 zero4 = (f32x4){0.f, 0.f, 0.f, 0.f};
            const char* xw = xch + (wr * 10 * 64 + lane) * 16;
            auto ldf = [&](int t) { return *(const s16x8*)(xw + t * 1024); };
            auto pairmm = [&](int pi, s16x8 kf, s16x8 vfr) {
                f32x4 dd = __builtin_amdgcn_mfma_f32_16x16x32_bf16(vfr, kf, zero4, 0, 0, 0);
                *(f32x4*)(red + pi * 256 + r16 * 16 + 4 * g) = dd;
            };
            auto zmm = [&](int kt2, s16x8 kf) {
                f32x4 zz = __builtin_amdgcn_mfma_f32_16x16x32_bf16(kf, ones, zero4, 0, 0, 0);
                if (r16 == 0) *(f32x4*)(red + 13 * 256 + kt2 * 16 + 4 * g) = zz;
            };
            if (wc == 0) {
                s16x8 k0 = ldf(0), k1 = ldf(1), k2 = ldf(2), v0 = ldf(5), v1 = ldf(6), v2 = ldf(7);
                pairmm(0, k0, v0); pairmm(1, k0, v1); zmm(0, k0);
                pairmm(2, k1, v0); pairmm(3, k1, v1); pairmm(4, k1, v2); zmm(1, k1);
                pairmm(5, k2, v1); pairmm(6, k2, v2);
            } else {
                s16x8 k2 = ldf(2), k3 = ldf(3), k4 = ldf(4), v2 = ldf(7), v3 = ldf(8), v4 = ldf(9);
                pairmm(7, k2, v3); zmm(2, k2);
                pairmm(8, k3, v2); pairmm(9, k3, v3); pairmm(10, k3, v4); zmm(3, k3);
                pairmm(11, k4, v3); pairmm(12, k4, v4); zmm(4, k4);
            }
        }
        __syncthreads();
        const f32x4* rb = (const f32x4*)smem;
        f32x4* dst = (f32x4*)(part + ((size_t)rt * 8 + (cg - 4)) * PSZ);
        for (int i = tid; i < PSZ / 4; i += 640)
            dst[i] = (rb[i] + rb[PSZ / 4 + i]) + (rb[2 * (PSZ / 4) + i] + rb[3 * (PSZ / 4) + i]);
    }
    STAMP(0,12);
}

__global__ __launch_bounds__(640) void k1_kernel(const u16* __restrict__ x, const u16* __restrict__ w,
                                                 u16* __restrict__ pq, float* __restrict__ part,
                                                 unsigned* __restrict__ cnt) {
    __shared__ __attribute__((aligned(16))) char smem[K1_NST * K1_STAGE];
    int b = blockIdx.x;
    if (b == 0 && threadIdx.x < 8) cnt[threadIdx.x * 64] = 0u;
    int xcd = b & 7, idx = b >> 3;
    int rt = (xcd >> 1) * 4 + (idx & 3);
    int cg = (xcd & 1) * 6 + (idx >> 2);
    if (cg < 4) k1_body<true>(x, w, pq, part, smem, rt, cg);
    else        k1_body<false>(x, w, pq, part, smem, rt, cg);
}

constexpr int OST = 648;
constexpr int K2_O_BYTES = 64 * OST * 2;
constexpr int K2_CH_BYTES = 128 * 80 * 2;
typedef __attribute__((ext_vector_type(16))) float f32x16;
__device__ __forceinline__ int wo_col(int krow, int cc) {
    int t = (cc >> 1) + 5 - ((krow >> 3) & 1);
    t = t >= 5 ? t - 5 : t;
    return t * 16 + (cc & 1) * 8;
}

__global__ __launch_bounds__(640) void k2_kernel(const u16* __restrict__ pq, const float* __restrict__ part,
                                                 u16* kvfrag, unsigned* cnt,
                                                 const u16* __restrict__ wo, u16* __restrict__ out) {
    __shared__ __attribute__((aligned(16))) char smem[K2_O_BYTES + 3 * K2_CH_BYTES];
    const int tid = threadIdx.x, lane = tid & 63;
    const int wv = __builtin_amdgcn_readfirstlane(tid >> 6);
    const int g = lane >> 4, r16 = lane & 15;
    const int b = blockIdx.x;
    const int xcd = b & 7, idx = b >> 3;
    const int rt = xcd * 4 + (idx & 3), ct = idx >> 2;
    const int row0 = rt * 64, col0 = ct * 80;
    char* oimg = smem;
    char* wbuf = smem + K2_O_BYTES;

    if (wv >= 8) {
        const int ld = wv - 8;
        if (b < 32) { __syncthreads(); __syncthreads(); __syncthreads(); }
#pragma unroll
        for (int ch = 0; ch < 3; ++ch)
#pragma unroll
            for (int i = 0; i < 10; ++i) {
                int c = (ld * 10 + i) * 64 + lane;
                const u16* src = wo + (size_t)(ch * 128 + c / 10) * D_ + col0 + wo_col(c / 10, c % 10);
                __builtin_amdgcn_global_load_lds((const unsigned*)src,
                    (unsigned*)(wbuf + ch * K2_CH_BYTES + (ld * 10 + i) * 1024 + lane * 16), 16, 0, 0);
            }
        u32x4 hold[20];
#pragma unroll
        for (int i = 0; i < 20; ++i) {
            int c = i * 64 + lane;
            hold[i] = *(const u32x4*)(wo + (size_t)((3 + ld) * 128 + c / 10) * D_ + col0 + wo_col(c / 10, c % 10));
        }
        WAIT_V(0);
        BAR();
        BAR();
        BAR();
        if (ld == 0) {
#pragma unroll
            for (int i = 0; i < 20; ++i) *(u32x4*)(wbuf + i * 1024 + lane * 16) = hold[i];
        }
        WAIT_L(0);
        BAR();
        if (ld == 1) {
#pragma unroll
            for (int i = 0; i < 20; ++i) *(u32x4*)(wbuf + K2_CH_BYTES + i * 1024 + lane * 16) = hold[i];
        }
        WAIT_L(0);
        BAR();
        BAR();
        return;
    }

    STAMP(2,0);
    float psum = 0.f;
    if (b < 32 && tid < 420) {
        int grp = b >> 2, hh = b & 3;
        int pidx;
        if (tid < 400) {
            int d = hh * 20 + tid / 20, e = hh * 20 + tid % 20;
            int kt = d >> 4, vt = e >> 4;
            int pi = kt == 0 ? vt : (3 * kt - 1 + (vt - kt + 1));
            pidx = pi * 256 + (d & 15) * 16 + (e & 15);
        } else {
            pidx = 13 * 256 + hh * 20 + (tid - 400);
        }
        float v[NRT];
#pragma unroll
        for (int p = 0; p < NRT; ++p) v[p] = part[((size_t)p * 8 + grp) * PSZ + pidx];
#pragma unroll
        for (int p = 0; p < NRT; ++p) psum += v[p];
    }
    u32x4 pv[10];
#pragma unroll
    for (int j = 0; j < 10; ++j) {
        int c = j * 512 + tid;
        int row = c / 80, cc = c % 80;
        pv[j] = *(const u32x4*)(pq + (size_t)(row0 + row) * D_ + cc * 8);
    }

    if (b < 32) {
        u16* red = (u16*)wbuf;
        if (tid < 128) *(u32x4*)(red + tid * 8) = (u32x4){0u, 0u, 0u, 0u};
        __syncthreads();
        if (tid < 400) red[(tid % 20) * 32 + tid / 20] = f2bf(psum);
        else if (tid < 420) red[20 * 32 + (tid - 400)] = f2bf(psum);
        __syncthreads();
        if (tid < 128) {
            int ks = tid >> 6;
            u32x4 ov = *(const u32x4*)(red + (lane & 31) * 32 + 16 * ks + 8 * (lane >> 5));
            unsigned long long* dst = (unsigned long long*)(kvfrag + ((size_t)(b * 2 + ks) * 64 + lane) * 8);
            __hip_atomic_store(dst, (unsigned long long)ov[0] | ((unsigned long long)ov[1] << 32), __ATOMIC_RELAXED, __HIP_MEMORY_SCOPE_AGENT);
            __hip_atomic_store(dst + 1, (unsigned long long)ov[2] | ((unsigned long long)ov[3] << 32), __ATOMIC_RELAXED, __HIP_MEMORY_SCOPE_AGENT);
        }
        WAIT_V(0);
        __syncthreads();
        if (tid < 8) __hip_atomic_fetch_add(cnt + tid * 64, 1u, __ATOMIC_RELAXED, __HIP_MEMORY_SCOPE_AGENT);
        STAMP(1,0);
    }

#pragma unroll
    for (int j = 0; j < 10; ++j) {
        int c = j * 512 + tid;
        int row = c / 80, cc = c % 80;
        *(u32x4*)(oimg + row * (OST * 2) + cc * 16) = pv[j];
    }
    if (tid < 64) *(u32x4*)(oimg + tid * (OST * 2) + 1280) = (u32x4){0u, 0u, 0u, 0u};

    if (tid == 0) {
        unsigned spins = 0;
        while (__hip_atomic_load(cnt + (b & 7) * 64, __ATOMIC_RELAXED, __HIP_MEMORY_SCOPE_AGENT) < 32u) {
            __builtin_amdgcn_s_sleep(4);
            if (++spins > (1u << 24)) break;
        }
        __builtin_amdgcn_fence(__ATOMIC_ACQUIRE, "wavefront");
    }
    WAIT_L(0);
    BAR();
    STAMP(2,8);

    s16x8 a0[4], a1[4];
    {
        const int row = lane & 31;
#pragma unroll
        for (int hh = 0; hh < 4; ++hh) {
            int h = wv * 4 + hh;
            u32x4 v0 = (u32x4){0u, 0u, 0u, 0u}, v1 = (u32x4){0u, 0u, 0u, 0u};
            if (row <= 20) {
                const unsigned long long* s0 = (const unsigned long long*)(kvfrag + ((size_t)(h * 2 + 0) * 64 + lane) * 8);
                unsigned long long t0 = __hip_atomic_load(s0, __ATOMIC_RELAXED, __HIP_MEMORY_SCOPE_AGENT);
                unsigned long long t1 = __hip_atomic_load(s0 + 1, __ATOMIC_RELAXED, __HIP_MEMORY_SCOPE_AGENT);
                v0 = (u32x4){(unsigned)t0, (unsigned)(t0 >> 32), (unsigned)t1, (unsigned)(t1 >> 32)};
                if (lane < 32) {
                    const unsigned long long* s1 = (const unsigned long long*)(kvfrag + ((size_t)(h * 2 + 1) * 64 + lane) * 8);
                    unsigned long long t2 = __hip_atomic_load(s1, __ATOMIC_RELAXED, __HIP_MEMORY_SCOPE_AGENT);
                    unsigned long long t3 = __hip_atomic_load(s1 + 1, __ATOMIC_RELAXED, __HIP_MEMORY_SCOPE_AGENT);
                    v1 = (u32x4){(unsigned)t2, (unsigned)(t2 >> 32), (unsigned)t3, (unsigned)(t3 >> 32)};
                }
            }
            a0[hh] = __builtin_bit_cast(s16x8, v0);
            a1[hh] = __builtin_bit_cast(s16x8, v1);
        }
    }
    STAMP(2,9);
    {
        const int hl = lane >> 5;
        char* rbase = oimg + (lane & 31) * (OST * 2) + wv * 160 + hl * 16;
        char* wbase = oimg + (lane & 31) * (OST * 2) + wv * 160 + hl * 8;
        f32x16 zero16;
#pragma unroll
        for (int i = 0; i < 16; ++i) zero16[i] = 0.f;
#pragma unroll
        for (int hh = 0; hh < 4; ++hh) {
#pragma unroll
            for (int sh = 0; sh < 2; ++sh) {
                const char* rp = rbase + sh * 32 * (OST * 2) + hh * 40;
                u32x2 l0 = *(const u32x2*)(rp), l1 = *(const u32x2*)(rp + 8);
                u32x2 l2 = *(const u32x2*)(rp + 32), l3 = *(const u32x2*)(rp + 40);
                u32x4 b0 = (u32x4){l0[0], l0[1], l1[0], l1[1]};
                u32x4 b1 = (u32x4){l2[0], l2[1], l3[0], l3[1]};
                f32x16 dd = __builtin_amdgcn_mfma_f32_32x32x16_bf16(a0[hh], __builtin_bit_cast(s16x8, b0), zero16, 0, 0, 0);
                dd = __builtin_amdgcn_mfma_f32_32x32x16_bf16(a1[hh], __builtin_bit_cast(s16x8, b1), dd, 0, 0, 0);
                unsigned du = __float_as_uint(dd[8]);
                auto sw = __builtin_amdgcn_permlane32_swap(du, du, false, false);
                float den = __uint_as_float(sw[1]);
                den = rbf(rbf(den) + 1e-6f);
                float inv = __builtin_amdgcn_rcpf(den);
                char* wp = wbase + sh * 32 * (OST * 2) + hh * 40;
#pragma unroll
                for (int q = 0; q < 3; ++q) {
                    u32x2 v;
                    v[0] = pack2(rbf(dd[4 * q + 0]) * inv, rbf(dd[4 * q + 1]) * inv);
                    v[1] = pack2(rbf(dd[4 * q + 2]) * inv, rbf(dd[4 * q + 3]) * inv);
                    if (q < 2) *(u32x2*)(wp + q * 16) = v;
                    else if (hl == 0) *(u32x2*)(wp + 32) = v;
                }
            }
        }
    }
    STAMP(2,1);

    const int wr = wv & 3, wc = wv >> 2;
    const f32x4 zero4 = (f32x4){0.f, 0.f, 0.f, 0.f};
    f32x4 acc[3];
#pragma unroll
    for (int n = 0; n < 3; ++n) acc[n] = zero4;
    const int q4 = (lane & 15) >> 2, p4 = lane & 3;
    const int w_off0 = (8 * g + q4) * 160 + p4 * 8;
    int t_off[3];
#pragma unroll
    for (int n = 0; n < 3; ++n) { int u = wc * 3 + n + (g & 1); t_off[n] = (u >= 5 ? u - 5 : u) * 32; }
    const int orow = wr * 16 + r16;
    const char* obase = oimg + orow * (OST * 2) + g * 16;

    auto phase_b = [&](auto NT) {
        constexpr int nt = decltype(NT)::value;
#pragma unroll 1
        for (int ch = 0; ch < 5; ++ch) {
            WAIT_L(0);
            BAR();
            STAMP(2,2+ch);
            const char* wb = wbuf + (ch >= 3 ? ch - 3 : ch) * K2_CH_BYTES + w_off0;
            const char* ob = obase + ch * 256;
            s16x8 of[4], wf[4][nt];
#pragma unroll
            for (int ks = 0; ks < 4; ++ks) {
                of[ks] = *(const s16x8*)(ob + ks * 64);
#pragma unroll
                for (int n = 0; n < nt; ++n) {
                    const char* p = wb + (32 * ks) * 160 + t_off[n];
                    wf[ks][n] = cat8(tr_read(p), tr_read(p + 4 * 160));
                }
            }
#pragma unroll
            for (int ks = 0; ks < 4; ++ks)
#pragma unroll
                for (int n = 0; n < nt; ++n)
                    acc[n] = __builtin_amdgcn_mfma_f32_16x16x32_bf16(wf[ks][n], of[ks], acc[n], 0, 0, 0);
        }
    };
    if (wc == 0) phase_b(std::integral_constant<int, 3>{});
    else         phase_b(std::integral_constant<int, 2>{});
    STAMP(2,7);
    int s = row0 + orow;
#pragma unroll
    for (int n = 0; n < 3; ++n) {
        if (n == 2 && wc == 1) continue;
        u32x2 v;
        v[0] = pack2(acc[n][0], acc[n][1]);
        v[1] = pack2(acc[n][2], acc[n][3]);
        *(u32x2*)(out + (size_t)s * D_ + col0 + (wc * 3 + n) * 16 + 4 * g) = v;
    }
}

extern "C" void kernel_launch(void* const* d_in, const int* in_sizes, int n_in,
                              void* d_out, int out_size, void* d_ws, size_t ws_size,
                              hipStream_t stream) {
    const u16* x = (const u16*)d_in[0];
    const u16* wqkv = (const u16*)d_in[1];
    const u16* wo = (const u16*)d_in[2];
    u16* out = (u16*)d_out;
    char* ws = (char*)d_ws;
    u16* pq = (u16*)ws;
    float* part = (float*)(ws + 4u * 1024 * 1024);
    u16* kvfrag = (u16*)(ws + 7u * 1024 * 1024);
    unsigned* cnt = (unsigned*)(ws + 8u * 1024 * 1024);
    k1_kernel<<<192, 640, 0, stream>>>(x, wqkv, pq, part, cnt);
    k2_kernel<<<256, 640, 0, stream>>>(pq, part, kvfrag, cnt, wo, out);
}
```

```cpp
#include <hip/hip_runtime.h>
#include <hip/hip_bf16.h>
#include <stdint.h>
#include <type_traits>

typedef unsigned short u16;
typedef __attribute__((ext_vector_type(8))) short s16x8;
typedef __attribute__((ext_vector_type(4))) short s16x4;
typedef __attribute__((ext_vector_type(4))) float f32x4;
typedef __attribute__((ext_vector_type(2))) unsigned int u32x2;
typedef __attribute__((ext_vector_type(4))) unsigned int u32x4;

constexpr int S_ = 2048, D_ = 640, H_ = 32, DH_ = 20, N3_ = 1920;
constexpr int NRT = 16;
constexpr int PSZ = 13 * 256 + 80;

#define LDS_AS __attribute__((address_space(3)))

#define STAMP(k,i)

__device__ __forceinline__ float bf2f(u16 u) { return __uint_as_float(((unsigned)u) << 16); }
__device__ __forceinline__ u16 f2bf(float f) { __bf16 b = (__bf16)f; return __builtin_bit_cast(u16, b); }
__device__ __forceinline__ float rbf(float f) { return bf2f(f2bf(f)); }
__device__ __forceinline__ unsigned pack2(float a, float b) { return (unsigned)f2bf(a) | ((unsigned)f2bf(b) << 16); }
__device__ __forceinline__ float phi(float acc) {
    float t = rbf(acc);
    float p = t > 0.f ? t + 1.0f : __expf(t);
    return rbf(rbf(p) + 1e-6f);
}

#define WAIT_V(n) asm volatile("s_waitcnt vmcnt(" #n ")" ::: "memory")
#define WAIT_L(n) asm volatile("s_waitcnt lgkmcnt(" #n ")" ::: "memory")
#define BAR() __builtin_amdgcn_s_barrier()

__device__ __forceinline__ s16x4 tr_read(const char* lds_ptr) {
    return __builtin_amdgcn_ds_read_tr16_b64_v4i16((LDS_AS s16x4*)lds_ptr);
}
__device__ __forceinline__ s16x8 cat8(s16x4 a, s16x4 b) {
    s16x8 r; r[0]=a[0]; r[1]=a[1]; r[2]=a[2]; r[3]=a[3]; r[4]=b[0]; r[5]=b[1]; r[6]=b[2]; r[7]=b[3]; return r;
}

constexpr int K1_A_BYTES = 128 * 64 * 2;
constexpr int K1_B_BYTES = 64 * 160 * 2;
constexpr int K1_STAGE = K1_A_BYTES + K1_B_BYTES;
constexpr int K1_NST = 4;

template <bool IS_Q>
__device__ __forceinline__ void k1_body(const u16* __restrict__ x, const u16* __restrict__ w,
                                        u16* __restrict__ pq, float* __restrict__ part,
                                        char* smem, int rt, int cg) {
    const int tid = threadIdx.x, lane = tid & 63;
    const int wv = __builtin_amdgcn_readfirstlane(tid >> 6);
    const int wr = wv & 3, wc = (wv >> 2) & 1;
    const int g = lane >> 4, r16 = lane & 15;
    const int row0 = rt * 128;

    f32x4 acc[2][5];
#pragma unroll
    for (int m = 0; m < 2; ++m)
#pragma unroll
        for (int n = 0; n < 5; ++n) acc[m][n] = (f32x4){0.f, 0.f, 0.f, 0.f};

    STAMP(0,0);
    if (wv >= 8) {
        const int ld = wv - 8;
        int a_src[8], b_src[10];
#pragma unroll
        for (int i = 0; i < 8; ++i) {
            int row = (ld * 8 + i) * 8 + (lane >> 3), slot = lane & 7;
            a_src[i] = (row0 + row) * D_ + ((slot ^ (row & 7)) * 8);
        }
#pragma unroll
        for (int i = 0; i < 10; ++i) {
            int c = (ld * 10 + i) * 64 + lane;
            int krow = c / 20, cc = c % 20;
            int sc = cc ^ (((krow >> 3) & 1) << 1);
            int col = sc * 8;
            int gcol;
            if (IS_Q) gcol = cg * 160 + col;
            else gcol = (col < 80) ? (640 + (cg - 4) * 80 + col) : (1280 + (cg - 4) * 80 + (col - 80));
            b_src[i] = krow * N3_ + gcol;
        }
        auto stage = [&](int st, int kt) {
            char* a_dst = smem + st * K1_STAGE + ld * 8192 + lane * 16;
            char* b_dst = smem + st * K1_STAGE + K1_A_BYTES + ld * 10240 + lane * 16;
#pragma unroll
            for (int i = 0; i < 8; ++i)
                __builtin_amdgcn_global_load_lds((const unsigned*)(x + a_src[i] + kt * 64), (unsigned*)(a_dst + i * 1024), 16, 0, 0);
#pragma unroll
            for (int i = 0; i < 10; ++i)
                __builtin_amdgcn_global_load_lds((const unsigned*)(w + b_src[i] + kt * 64 * N3_), (unsigned*)(b_dst + i * 1024), 16, 0, 0);
        };
        stage(0, 0); stage(1, 1); stage(2, 2);
        WAIT_V(36);
        BAR();
#pragma unroll 1
        for (int kt = 0; kt < 10; ++kt) {
            if (kt < 7) WAIT_V(18); else WAIT_V(0);
            BAR();
            if (kt + 3 < 10) stage((kt + 3) & 3, kt + 3);
        }
    } else {
        int a_off[2][2];
#pragma unroll
        for (int m = 0; m < 2; ++m)
#pragma unroll
            for (int ks = 0; ks < 2; ++ks) {
                int row = wr * 32 + m * 16 + r16;
                a_off[m][ks] = row * 128 + (((4 * ks + g) ^ (row & 7)) * 16);
            }
        const int q4 = (lane & 15) >> 2, p4 = lane & 3;
        int b_off[5];
#pragma unroll
        for (int n = 0; n < 5; ++n) {
            int tl = IS_Q ? wc * 5 + n : (wc == 0 ? (n < 3 ? n : n + 2) : (n < 2 ? n + 3 : n + 5));
            b_off[n] = (8 * g + q4) * 320 + p4 * 8 + ((tl ^ (g & 1)) * 32);
        }

        s16x8 af0[2], bq0[5], af1[2], bq1[5];
        auto rd = [&](s16x8* af, s16x8* bq, int kt, int ks) {
            const char* a_base = smem + (kt & 3) * K1_STAGE;
            const char* b_base = a_base + K1_A_BYTES;
#pragma unroll
            for (int m = 0; m < 2; ++m) af[m] = *(const s16x8*)(a_base + a_off[m][ks]);
#pragma unroll
            for (int n = 0; n < 5; ++n) {
                const char* p = b_base + b_off[n] + (32 * ks) * 320;
                bq[n] = cat8(tr_read(p), tr_read(p + 4 * 320));
            }
        };
        auto mm = [&](const s16x8* af, const s16x8* bq) {
#pragma unroll
            for (int n = 0; n < 5; ++n) {
#pragma unroll
                for (int m = 0; m < 2; ++m) {
                    if (IS_Q) acc[m][n] = __builtin_amdgcn_mfma_f32_16x16x32_bf16(bq[n], af[m], acc[m][n], 0, 0, 0);
                    else      acc[m][n] = __builtin_amdgcn_mfma_f32_16x16x32_bf16(af[m], bq[n], acc[m][n], 0, 0, 0);
                }
            }
        };
        BAR();
        rd(af0, bq0, 0, 0);
#pragma unroll 1
        for (int kt = 0; kt < 10; ++kt) {
            BAR();
            STAMP(0,1+kt);
            __builtin_amdgcn_s_waitcnt(0xC07F);
            __builtin_amdgcn_sched_barrier(0);
            rd(af1, bq1, kt, 1);
            __builtin_amdgcn_sched_barrier(0);
            mm(af0, bq0);
            __builtin_amdgcn_sched_barrier(0);
            rd(af0, bq0, kt + 1, 0);
            __builtin_amdgcn_sched_barrier(0);
            mm(af1, bq1);
            __builtin_amdgcn_sched_barrier(0);
        }
    }
    STAMP(0,11);

    if (IS_Q) {
        if (wv >= 8) return;
#pragma unroll
        for (int m = 0; m < 2; ++m) {
            int s = row0 + wr * 32 + m * 16 + r16;
#pragma unroll
            for (int n = 0; n < 5; ++n) {
                u32x2 v;
                v[0] = pack2(phi(acc[m][n][0]), phi(acc[m][n][1]));
                v[1] = pack2(phi(acc[m][n][2]), phi(acc[m][n][3]));
                *(u32x2*)(pq + (size_t)s * D_ + cg * 160 + wc * 80 + n * 16 + 4 * g) = v;
            }
        }
    } else {
        WAIT_L(0);
        BAR();
        char* xch = smem + 64 * 1024;
        if (wv < 8) {
            const int nk = wc == 0 ? 3 : 2;
#pragma unroll
            for (int n = 0; n < 5; ++n) {
                s16x8 fr;
#pragma unroll
                for (int j = 0; j < 4; ++j) {
                    float a0 = acc[0][n][j], a1 = acc[1][n][j];
                    if (n < 2 || (n == 2 && nk == 3)) { a0 = phi(a0); a1 = phi(a1); }
                    fr[j] = (short)f2bf(a0);
                    fr[4 + j] = (short)f2bf(a1);
                }
                int tl = wc == 0 ? (n < 3 ? n : n + 2) : (n < 2 ? n + 3 : n + 5);
                *(s16x8*)(xch + ((wr * 10 + tl) * 64 + lane) * 16) = fr;
            }
        }
        __syncthreads();
        if (wv < 8) {
            s16x8 ones;
#pragma unroll
            for (int j = 0; j < 8; ++j) ones[j] = (short)0x3F80;
            float* red = (float*)smem + wr * PSZ;
            const f32x4 zero4 = (f32x4){0.f, 0.f, 0.f, 0.f};
            const char* xw = xch + (wr * 10 * 64 + lane) * 16;
            auto ldf = [&](int t) { return *(const s16x8*)(xw + t * 1024); };
            auto pairmm = [&](int pi, s16x8 kf, s16x8 vfr) {
                f32x4 dd = __builtin_amdgcn_mfma_f32_16x16x32_bf16(vfr, kf, zero4, 0, 0, 0);
                *(f32x4*)(red + pi * 256 + r16 * 16 + 4 * g) = dd;
            };
            auto zmm = [&](int kt2, s16x8 kf) {
                f32x4 zz = __builtin_amdgcn_mfma_f32_16x16x32_bf16(kf, ones, zero4, 0, 0, 0);
                if (r16 == 0) *(f32x4*)(red + 13 * 256 + kt2 * 16 + 4 * g) = zz;
            };
            if (wc == 0) {
                s16x8 k0 = ldf(0), k1 = ldf(1), k2 = ldf(2), v0 = ldf(5), v1 = ldf(6), v2 = ldf(7);
                pairmm(0, k0, v0); pairmm(1, k0, v1); zmm(0, k0);
                pairmm(2, k1, v0); pairmm(3, k1, v1); pairmm(4, k1, v2); zmm(1, k1);
                pairmm(5, k2, v1); pairmm(6, k2, v2);
            } else {
                s16x8 k2 = ldf(2), k3 = ldf(3), k4 = ldf(4), v2 = ldf(7), v3 = ldf(8), v4 = ldf(9);
                pairmm(7, k2, v3); zmm(2, k2);
                pairmm(8, k3, v2); pairmm(9, k3, v3); pairmm(10, k3, v4); zmm(3, k3);
                pairmm(11, k4, v3); pairmm(12, k4, v4); zmm(4, k4);
            }
        }
        __syncthreads();
        const f32x4* rb = (const f32x4*)smem;
        f32x4* dst = (f32x4*)(part + ((size_t)rt * 8 + (cg - 4)) * PSZ);
        for (int i = tid; i < PSZ / 4; i += 640)
            dst[i] = (rb[i] + rb[PSZ / 4 + i]) + (rb[2 * (PSZ / 4) + i] + rb[3 * (PSZ / 4) + i]);
    }
    STAMP(0,12);
}

__global__ __launch_bounds__(640) void k1_kernel(const u16* __restrict__ x, const u16* __restrict__ w,
                                                 u16* __restrict__ pq, float* __restrict__ part,
                                                 unsigned* __restrict__ cnt,
                                                 const u16* __restrict__ wo, u16* __restrict__ woT) {
    __shared__ __attribute__((aligned(16))) char smem[K1_NST * K1_STAGE];
    int b = blockIdx.x;
    if (b >= 192) {
        const int k0 = (b - 192) * 16, tid = threadIdx.x;
        u16* t = (u16*)smem;
#pragma unroll
        for (int j = 0; j < 2; ++j) {
            int c = j * 640 + tid, r = c / 80, cc = c % 80;
            *(u32x4*)(t + r * 648 + cc * 8) = *(const u32x4*)(wo + (size_t)(k0 + r) * D_ + cc * 8);
        }
        __syncthreads();
        u32x4 o0, o1;
#pragma unroll
        for (int j = 0; j < 4; ++j) {
            o0[j] = (unsigned)t[(2 * j) * 648 + tid] | ((unsigned)t[(2 * j + 1) * 648 + tid] << 16);
            o1[j] = (unsigned)t[(8 + 2 * j) * 648 + tid] | ((unsigned)t[(9 + 2 * j) * 648 + tid] << 16);
        }
        *(u32x4*)(woT + (size_t)tid * D_ + k0) = o0;
        *(u32x4*)(woT + (size_t)tid * D_ + k0 + 8) = o1;
        return;
    }
    if (b == 0 && threadIdx.x < 8) cnt[threadIdx.x * 64] = 0u;
    int xcd = b & 7, idx = b >> 3;
    int rt = (xcd >> 1) * 4 + (idx & 3);
    int cg = (xcd & 1) * 6 + (idx >> 2);
    if (cg < 4) k1_body<true>(x, w, pq, part, smem, rt, cg);
    else        k1_body<false>(x, w, pq, part, smem, rt, cg);
}

constexpr int OST = 648;
constexpr int K2_O_BYTES = 64 * OST * 2;
constexpr int K2_CH_BYTES = 128 * 80 * 2;
typedef __attribute__((ext_vector_type(16))) float f32x16;
__device__ __forceinline__ int wo_col(int krow, int cc) {
    int t = (cc >> 1) + 5 - ((krow >> 3) & 1);
    t = t >= 5 ? t - 5 : t;
    return t * 16 + (cc & 1) * 8;
}

__global__ __launch_bounds__(640) void k2_kernel(const u16* __restrict__ pq, const float* __restrict__ part,
                                                 u16* kvfrag, unsigned* cnt,
                                                 const u16* __restrict__ wo, u16* __restrict__ out) {
    __shared__ __attribute__((aligned(16))) char smem[K2_O_BYTES + 3 * K2_CH_BYTES];
    const int tid = threadIdx.x, lane = tid & 63;
    const int wv = __builtin_amdgcn_readfirstlane(tid >> 6);
    const int g = lane >> 4, r16 = lane & 15;
    const int b = blockIdx.x;
    const int xcd = b & 7, idx = b >> 3;
    const int rt = xcd * 4 + (idx & 3), ct = idx >> 2;
    const int row0 = rt * 64, col0 = ct * 80;
    char* oimg = smem;
    char* wbuf = smem + K2_O_BYTES;

    if (wv >= 8) {
        const int ld = wv - 8;
        if (b < 32) { __syncthreads(); __syncthreads(); __syncthreads(); }
#pragma unroll
        for (int ch = 0; ch < 3; ++ch)
#pragma unroll
            for (int i = 0; i < 10; ++i) {
                int row = (ld * 10 + i) * 4 + (lane >> 4);
                const u16* src = wo + (size_t)(col0 + row) * D_ + ch * 128 + (((lane & 15) ^ (row & 15)) * 8);
                __builtin_amdgcn_global_load_lds((const unsigned*)src,
                    (unsigned*)(wbuf + ch * K2_CH_BYTES + (ld * 10 + i) * 1024 + lane * 16), 16, 0, 0);
            }
        u32x4 hold[20];
#pragma unroll
        for (int i = 0; i < 20; ++i) {
            int row = i * 4 + (lane >> 4);
            hold[i] = *(const u32x4*)(wo + (size_t)(col0 + row) * D_ + (3 + ld) * 128 + (((lane & 15) ^ (row & 15)) * 8));
        }
        WAIT_V(0);
        BAR();
        BAR();
        BAR();
        if (ld == 0) {
#pragma unroll
            for (int i = 0; i < 20; ++i) *(u32x4*)(wbuf + i * 1024 + lane * 16) = hold[i];
        }
        WAIT_L(0);
        BAR();
        if (ld == 1) {
#pragma unroll
            for (int i = 0; i < 20; ++i) *(u32x4*)(wbuf + K2_CH_BYTES + i * 1024 + lane * 16) = hold[i];
        }
        WAIT_L(0);
        BAR();
        BAR();
        return;
    }

    STAMP(2,0);
    float psum = 0.f;
    if (b < 32 && tid < 420) {
        int grp = b >> 2, hh = b & 3;
        int pidx;
        if (tid < 400) {
            int d = hh * 20 + tid / 20, e = hh * 20 + tid % 20;
            int kt = d >> 4, vt = e >> 4;
            int pi = kt == 0 ? vt : (3 * kt - 1 + (vt - kt + 1));
            pidx = pi * 256 + (d & 15) * 16 + (e & 15);
        } else {
            pidx = 13 * 256 + hh * 20 + (tid - 400);
        }
        float v[NRT];
#pragma unroll
        for (int p = 0; p < NRT; ++p) v[p] = part[((size_t)p * 8 + grp) * PSZ + pidx];
#pragma unroll
        for (int p = 0; p < NRT; ++p) psum += v[p];
    }
    u32x4 pv[10];
#pragma unroll
    for (int j = 0; j < 10; ++j) {
        int c = j * 512 + tid;
        int row = c / 80, cc = c % 80;
        pv[j] = *(const u32x4*)(pq + (size_t)(row0 + row) * D_ + cc * 8);
    }

    if (b < 32) {
        u16* red = (u16*)wbuf;
        if (tid < 128) *(u32x4*)(red + tid * 8) = (u32x4){0u, 0u, 0u, 0u};
        __syncthreads();
        if (tid < 400) red[(tid % 20) * 32 + tid / 20] = f2bf(psum);
        else if (tid < 420) red[20 * 32 + (tid - 400)] = f2bf(psum);
        __syncthreads();
        if (tid < 128) {
            int ks = tid >> 6;
            u32x4 ov = *(const u32x4*)(red + (lane & 31) * 32 + 16 * ks + 8 * (lane >> 5));
            unsigned long long* dst = (unsigned long long*)(kvfrag + ((size_t)(b * 2 + ks) * 64 + lane) * 8);
            __hip_atomic_store(dst, (unsigned long long)ov[0] | ((unsigned long long)ov[1] << 32), __ATOMIC_RELAXED, __HIP_MEMORY_SCOPE_AGENT);
            __hip_atomic_store(dst + 1, (unsigned long long)ov[2] | ((unsigned long long)ov[3] << 32), __ATOMIC_RELAXED, __HIP_MEMORY_SCOPE_AGENT);
        }
        WAIT_V(0);
        __syncthreads();
        if (tid < 8) __hip_atomic_fetch_add(cnt + tid * 64, 1u, __ATOMIC_RELAXED, __HIP_MEMORY_SCOPE_AGENT);
        STAMP(1,0);
    }

#pragma unroll
    for (int j = 0; j < 10; ++j) {
        int c = j * 512 + tid;
        int row = c / 80, cc = c % 80;
        *(u32x4*)(oimg + row * (OST * 2) + cc * 16) = pv[j];
    }
    if (tid < 64) *(u32x4*)(oimg + tid * (OST * 2) + 1280) = (u32x4){0u, 0u, 0u, 0u};

    if (tid == 0) {
        unsigned spins = 0;
        while (__hip_atomic_load(cnt + (b & 7) * 64, __ATOMIC_RELAXED, __HIP_MEMORY_SCOPE_AGENT) < 32u) {
            __builtin_amdgcn_s_sleep(4);
            if (++spins > (1u << 24)) break;
        }
        __builtin_amdgcn_fence(__ATOMIC_ACQUIRE, "wavefront");
    }
    WAIT_L(0);
    BAR();
    STAMP(2,8);

    s16x8 a0[4], a1[4];
    {
        const int row = lane & 31;
#pragma unroll
        for (int hh = 0; hh < 4; ++hh) {
            int h = wv * 4 + hh;
            u32x4 v0 = (u32x4){0u, 0u, 0u, 0u}, v1 = (u32x4){0u, 0u, 0u, 0u};
            if (row <= 20) {
                const unsigned long long* s0 = (const unsigned long long*)(kvfrag + ((size_t)(h * 2 + 0) * 64 + lane) * 8);
                unsigned long long t0 = __hip_atomic_load(s0, __ATOMIC_RELAXED, __HIP_MEMORY_SCOPE_AGENT);
                unsigned long long t1 = __hip_atomic_load(s0 + 1, __ATOMIC_RELAXED, __HIP_MEMORY_SCOPE_AGENT);
                v0 = (u32x4){(unsigned)t0, (unsigned)(t0 >> 32), (unsigned)t1, (unsigned)(t1 >> 32)};
                if (lane < 32) {
                    const unsigned long long* s1 = (const unsigned long long*)(kvfrag + ((size_t)(h * 2 + 1) * 64 + lane) * 8);
                    unsigned long long t2 = __hip_atomic_load(s1, __ATOMIC_RELAXED, __HIP_MEMORY_SCOPE_AGENT);
                    unsigned long long t3 = __hip_atomic_load(s1 + 1, __ATOMIC_RELAXED, __HIP_MEMORY_SCOPE_AGENT);
                    v1 = (u32x4){(unsigned)t2, (unsigned)(t2 >> 32), (unsigned)t3, (unsigned)(t3 >> 32)};
                }
            }
            a0[hh] = __builtin_bit_cast(s16x8, v0);
            a1[hh] = __builtin_bit_cast(s16x8, v1);
        }
    }
    STAMP(2,9);
    {
        const int hl = lane >> 5;
        char* rbase = oimg + (lane & 31) * (OST * 2) + wv * 160 + hl * 16;
        char* wbase = oimg + (lane & 31) * (OST * 2) + wv * 160 + hl * 8;
        f32x16 zero16;
#pragma unroll
        for (int i = 0; i < 16; ++i) zero16[i] = 0.f;
#pragma unroll
        for (int hh = 0; hh < 4; ++hh) {
#pragma unroll
            for (int sh = 0; sh < 2; ++sh) {
                const char* rp = rbase + sh * 32 * (OST * 2) + hh * 40;
                u32x2 l0 = *(const u32x2*)(rp), l1 = *(const u32x2*)(rp + 8);
                u32x2 l2 = *(const u32x2*)(rp + 32), l3 = *(const u32x2*)(rp + 40);
                u32x4 b0 = (u32x4){l0[0], l0[1], l1[0], l1[1]};
                u32x4 b1 = (u32x4){l2[0], l2[1], l3[0], l3[1]};
                f32x16 dd = __builtin_amdgcn_mfma_f32_32x32x16_bf16(a0[hh], __builtin_bit_cast(s16x8, b0), zero16, 0, 0, 0);
                dd = __builtin_amdgcn_mfma_f32_32x32x16_bf16(a1[hh], __builtin_bit_cast(s16x8, b1), dd, 0, 0, 0);
                unsigned du = __float_as_uint(dd[8]);
                auto sw = __builtin_amdgcn_permlane32_swap(du, du, false, false);
                float den = __uint_as_float(sw[1]);
                den = rbf(rbf(den) + 1e-6f);
                float inv = __builtin_amdgcn_rcpf(den);
                char* wp = wbase + sh * 32 * (OST * 2) + hh * 40;
#pragma unroll
                for (int q = 0; q < 3; ++q) {
                    u32x2 v;
                    v[0] = pack2(rbf(dd[4 * q + 0]) * inv, rbf(dd[4 * q + 1]) * inv);
                    v[1] = pack2(rbf(dd[4 * q + 2]) * inv, rbf(dd[4 * q + 3]) * inv);
                    if (q < 2) *(u32x2*)(wp + q * 16) = v;
                    else if (hl == 0) *(u32x2*)(wp + 32) = v;
                }
            }
        }
    }
    STAMP(2,1);

    const int wr = wv & 3, wc = wv >> 2;
    const f32x4 zero4 = (f32x4){0.f, 0.f, 0.f, 0.f};
    f32x4 acc[3];
#pragma unroll
    for (int n = 0; n < 3; ++n) acc[n] = zero4;
    int w_off[4];
#pragma unroll
    for (int ks = 0; ks < 4; ++ks) w_off[ks] = (wc * 48 + r16) * 256 + (((4 * ks + g) ^ r16) * 16);
    const int orow = wr * 16 + r16;
    const char* obase = oimg + orow * (OST * 2) + g * 16;

    auto phase_b = [&](auto NT) {
        constexpr int nt = decltype(NT)::value;
#pragma unroll 1
        for (int ch = 0; ch < 5; ++ch) {
            WAIT_L(0);
            BAR();
            STAMP(2,2+ch);
            const char* wb = wbuf + (ch >= 3 ? ch - 3 : ch) * K2_CH_BYTES;
            const char* ob = obase + ch * 256;
            s16x8 of[4], wf[4][nt];
#pragma unroll
            for (int ks = 0; ks < 4; ++ks) {
                of[ks] = *(const s16x8*)(ob + ks * 64);
#pragma unroll
                for (int n = 0; n < nt; ++n) {
                    wf[ks][n] = *(const s16x8*)(wb + w_off[ks] + n * 4096);
                }
            }
#pragma unroll
            for (int ks = 0; ks < 4; ++ks)
#pragma unroll
                for (int n = 0; n < nt; ++n)
                    acc[n] = __builtin_amdgcn_mfma_f32_16x16x32_bf16(wf[ks][n], of[ks], acc[n], 0, 0, 0);
        }
    };
    if (wc == 0) phase_b(std::integral_constant<int, 3>{});
    else         phase_b(std::integral_constant<int, 2>{});
    STAMP(2,7);
    int s = row0 + orow;
#pragma unroll
    for (int n = 0; n < 3; ++n) {
        if (n == 2 && wc == 1) continue;
        u32x2 v;
        v[0] = pack2(acc[n][0], acc[n][1]);
        v[1] = pack2(acc[n][2], acc[n][3]);
        *(u32x2*)(out + (size_t)s * D_ + col0 + (wc * 3 + n) * 16 + 4 * g) = v;
    }
}

extern "C" void kernel_launch(void* const* d_in, const int* in_sizes, int n_in,
                              void* d_out, int out_size, void* d_ws, size_t ws_size,
                              hipStream_t stream) {
    const u16* x = (const u16*)d_in[0];
    const u16* wqkv = (const u16*)d_in[1];
    const u16* wo = (const u16*)d_in[2];
    u16* out = (u16*)d_out;
    char* ws = (char*)d_ws;
    u16* pq = (u16*)ws;
    float* part = (float*)(ws + 4u * 1024 * 1024);
    u16* kvfrag = (u16*)(ws + 7u * 1024 * 1024);
    unsigned* cnt = (unsigned*)(ws + 8u * 1024 * 1024);
    u16* woT = (u16*)(ws + 10u * 1024 * 1024);
    k1_kernel<<<232, 640, 0, stream>>>(x, wqkv, pq, part, cnt, wo, woT);
    k2_kernel<<<256, 640, 0, stream>>>(pq, part, kvfrag, cnt, woT, out);
}
```

```cpp
#include <hip/hip_runtime.h>
#include <hip/hip_bf16.h>
#include <stdint.h>
#include <type_traits>

typedef unsigned short u16;
typedef __attribute__((ext_vector_type(8))) short s16x8;
typedef __attribute__((ext_vector_type(4))) short s16x4;
typedef __attribute__((ext_vector_type(4))) float f32x4;
typedef __attribute__((ext_vector_type(2))) unsigned int u32x2;
typedef __attribute__((ext_vector_type(4))) unsigned int u32x4;

constexpr int S_ = 2048, D_ = 640, H_ = 32, DH_ = 20, N3_ = 1920;
constexpr int NRT = 16;
constexpr int PSZ = 13 * 256 + 80;

#define LDS_AS __attribute__((address_space(3)))

#define STAMP(k,i)

__device__ __forceinline__ float bf2f(u16 u) { return __uint_as_float(((unsigned)u) << 16); }
__device__ __forceinline__ u16 f2bf(float f) { __bf16 b = (__bf16)f; return __builtin_bit_cast(u16, b); }
__device__ __forceinline__ float rbf(float f) { return bf2f(f2bf(f)); }
typedef __attribute__((ext_vector_type(2))) float f32x2;
typedef __attribute__((ext_vector_type(2))) __bf16 bf16x2;
__device__ __forceinline__ unsigned pack2(float a, float b) {
    f32x2 v = {a, b};
    return __builtin_bit_cast(unsigned, __builtin_convertvector(v, bf16x2));
}
__device__ __forceinline__ float lo2f(unsigned u) { return __uint_as_float(u << 16); }
__device__ __forceinline__ float hi2f(unsigned u) { return __uint_as_float(u & 0xffff0000u); }
__device__ __forceinline__ unsigned phi2(float a, float b) {
    unsigned t = pack2(a, b);
    float ta = lo2f(t), tb = hi2f(t);
    float pa = ta > 0.f ? ta + 1.0f : __expf(ta);
    float pb = tb > 0.f ? tb + 1.0f : __expf(tb);
    unsigned p = pack2(pa, pb);
    return pack2(lo2f(p) + 1e-6f, hi2f(p) + 1e-6f);
}
__device__ __forceinline__ float phi(float acc) {
    float t = rbf(acc);
    float p = t > 0.f ? t + 1.0f : __expf(t);
    return rbf(rbf(p) + 1e-6f);
}

#define WAIT_V(n) asm volatile("s_waitcnt vmcnt(" #n ")" ::: "memory")
#define WAIT_L(n) asm volatile("s_waitcnt lgkmcnt(" #n ")" ::: "memory")
#define BAR() __builtin_amdgcn_s_barrier()

__device__ __forceinline__ s16x4 tr_read(const char* lds_ptr) {
    return __builtin_amdgcn_ds_read_tr16_b64_v4i16((LDS_AS s16x4*)lds_ptr);
}
__device__ __forceinline__ s16x8 cat8(s16x4 a, s16x4 b) {
    s16x8 r; r[0]=a[0]; r[1]=a[1]; r[2]=a[2]; r[3]=a[3]; r[4]=b[0]; r[5]=b[1]; r[6]=b[2]; r[7]=b[3]; return r;
}

constexpr int K1_A_BYTES = 128 * 64 * 2;
constexpr int K1_B_BYTES = 64 * 160 * 2;
constexpr int K1_STAGE = K1_A_BYTES + K1_B_BYTES;
constexpr int K1_NST = 4;

template <bool IS_Q>
__device__ __forceinline__ void k1_body(const u16* __restrict__ x, const u16* __restrict__ w,
                                        u16* __restrict__ pq, float* __restrict__ part,
                                        char* smem, int rt, int cg) {
    const int tid = threadIdx.x, lane = tid & 63;
    const int wv = __builtin_amdgcn_readfirstlane(tid >> 6);
    const int wr = wv & 3, wc = (wv >> 2) & 1;
    const int g = lane >> 4, r16 = lane & 15;
    const int row0 = rt * 128;

    f32x4 acc[2][5];
#pragma unroll
    for (int m = 0; m < 2; ++m)
#pragma unroll
        for (int n = 0; n < 5; ++n) acc[m][n] = (f32x4){0.f, 0.f, 0.f, 0.f};

    STAMP(0,0);
    if (wv >= 8) {
        const int ld = wv - 8;
        int a_src[8], b_src[10];
#pragma unroll
        for (int i = 0; i < 8; ++i) {
            int row = (ld * 8 + i) * 8 + (lane >> 3), slot = lane & 7;
            a_src[i] = (row0 + row) * D_ + ((slot ^ (row & 7)) * 8);
        }
#pragma unroll
        for (int i = 0; i < 10; ++i) {
            int c = (ld * 10 + i) * 64 + lane;
            int krow = c / 20, cc = c % 20;
            int sc = cc ^ (((krow >> 3) & 1) << 1);
            int col = sc * 8;
            int gcol;
            if (IS_Q) gcol = cg * 160 + col;
            else gcol = (col < 80) ? (640 + (cg - 4) * 80 + col) : (1280 + (cg - 4) * 80 + (col - 80));
            b_src[i] = krow * N3_ + gcol;
        }
        auto stage = [&](int st, int kt) {
            char* a_dst = smem + st * K1_STAGE + ld * 8192 + lane * 16;
            char* b_dst = smem + st * K1_STAGE + K1_A_BYTES + ld * 10240 + lane * 16;
#pragma unroll
            for (int i = 0; i < 8; ++i)
                __builtin_amdgcn_global_load_lds((const unsigned*)(x + a_src[i] + kt * 64), (unsigned*)(a_dst + i * 1024), 16, 0, 0);
#pragma unroll
            for (int i = 0; i < 10; ++i)
                __builtin_amdgcn_global_load_lds((const unsigned*)(w + b_src[i] + kt * 64 * N3_), (unsigned*)(b_dst + i * 1024), 16, 0, 0);
        };
        stage(0, 0); stage(1, 1); stage(2, 2);
        WAIT_V(36);
        BAR();
#pragma unroll 1
        for (int kt = 0; kt < 10; ++kt) {
            if (kt < 7) WAIT_V(18); else WAIT_V(0);
            BAR();
            if (kt + 3 < 10) stage((kt + 3) & 3, kt + 3);
        }
    } else {
        int a_off[2][2];
#pragma unroll
        for (int m = 0; m < 2; ++m)
#pragma unroll
            for (int ks = 0; ks < 2; ++ks) {
                int row = wr * 32 + m * 16 + r16;
                a_off[m][ks] = row * 128 + (((4 * ks + g) ^ (row & 7)) * 16);
            }
        const int q4 = (lane & 15) >> 2, p4 = lane & 3;
        int b_off[5];
#pragma unroll
        for (int n = 0; n < 5; ++n) {
            int tl = IS_Q ? wc * 5 + n : (wc == 0 ? (n < 3 ? n : n + 2) : (n < 2 ? n + 3 : n + 5));
            b_off[n] = (8 * g + q4) * 320 + p4 * 8 + ((tl ^ (g & 1)) * 32);
        }

        s16x8 af0[2], bq0[5], af1[2], bq1[5];
        auto rd = [&](s16x8* af, s16x8* bq, int kt, int ks) {
            const char* a_base = smem + (kt & 3) * K1_STAGE;
            const char* b_base = a_base + K1_A_BYTES;
#pragma unroll
            for (int m = 0; m < 2; ++m) af[m] = *(const s16x8*)(a_base + a_off[m][ks]);
#pragma unroll
            for (int n = 0; n < 5; ++n) {
                const char* p = b_base + b_off[n] + (32 * ks) * 320;
                bq[n] = cat8(tr_read(p), tr_read(p + 4 * 320));
            }
        };
        auto mm = [&](const s16x8* af, const s16x8* bq) {
#pragma unroll
            for (int n = 0; n < 5; ++n) {
#pragma unroll
                for (int m = 0; m < 2; ++m) {
                    if (IS_Q) acc[m][n] = __builtin_amdgcn_mfma_f32_16x16x32_bf16(bq[n], af[m], acc[m][n], 0, 0, 0);
                    else      acc[m][n] = __builtin_amdgcn_mfma_f32_16x16x32_bf16(af[m], bq[n], acc[m][n], 0, 0, 0);
                }
            }
        };
        BAR();
        rd(af0, bq0, 0, 0);
#pragma unroll 1
        for (int kt = 0; kt < 10; ++kt) {
            BAR();
            STAMP(0,1+kt);
            __builtin_amdgcn_s_waitcnt(0xC07F);
            __builtin_amdgcn_sched_barrier(0);
            rd(af1, bq1, kt, 1);
            __builtin_amdgcn_sched_barrier(0);
            mm(af0, bq0);
            __builtin_amdgcn_sched_barrier(0);
            rd(af0, bq0, kt + 1, 0);
            __builtin_amdgcn_sched_barrier(0);
            mm(af1, bq1);
            __builtin_amdgcn_sched_barrier(0);
        }
    }
    STAMP(0,11);

    if (IS_Q) {
        if (wv >= 8) return;
#pragma unroll
        for (int m = 0; m < 2; ++m) {
            int s = row0 + wr * 32 + m * 16 + r16;
#pragma unroll
            for (int n = 0; n < 5; ++n) {
                u32x2 v;
                v[0] = phi2(acc[m][n][0], acc[m][n][1]);
                v[1] = phi2(acc[m][n][2], acc[m][n][3]);
                *(u32x2*)(pq + (size_t)s * D_ + cg * 160 + wc * 80 + n * 16 + 4 * g) = v;
            }
        }
    } else {
        WAIT_L(0);
        BAR();
        char* xch = smem + 64 * 1024;
        if (wv < 8) {
            const int nk = wc == 0 ? 3 : 2;
#pragma unroll
            for (int n = 0; n < 5; ++n) {
                u32x4 fr;
                if (n < 2 || (n == 2 && nk == 3)) {
                    fr[0] = phi2(acc[0][n][0], acc[0][n][1]); fr[1] = phi2(acc[0][n][2], acc[0][n][3]);
                    fr[2] = phi2(acc[1][n][0], acc[1][n][1]); fr[3] = phi2(acc[1][n][2], acc[1][n][3]);
                } else {
                    fr[0] = pack2(acc[0][n][0], acc[0][n][1]); fr[1] = pack2(acc[0][n][2], acc[0][n][3]);
                    fr[2] = pack2(acc[1][n][0], acc[1][n][1]); fr[3] = pack2(acc[1][n][2], acc[1][n][3]);
                }
                int tl = wc == 0 ? (n < 3 ? n : n + 2) : (n < 2 ? n + 3 : n + 5);
                *(u32x4*)(xch + ((wr * 10 + tl) * 64 + lane) * 16) = fr;
            }
        }
        __syncthreads();
        if (wv < 8) {
            s16x8 ones;
#pragma unroll
            for (int j = 0; j < 8; ++j) ones[j] = (short)0x3F80;
            float* red = (float*)smem + wr * PSZ;
            const f32x4 zero4 = (f32x4){0.f, 0.f, 0.f, 0.f};
            const char* xw = xch + (wr * 10 * 64 + lane) * 16;
            auto ldf = [&](int t) { return *(const s16x8*)(xw + t * 1024); };
            auto pairmm = [&](int pi, s16x8 kf, s16x8 vfr) {
                f32x4 dd = __builtin_amdgcn_mfma_f32_16x16x32_bf16(vfr, kf, zero4, 0, 0, 0);
                *(f32x4*)(red + pi * 256 + r16 * 16 + 4 * g) = dd;
            };
            auto zmm = [&](int kt2, s16x8 kf) {
                f32x4 zz = __builtin_amdgcn_mfma_f32_16x16x32_bf16(kf, ones, zero4, 0, 0, 0);
                if (r16 == 0) *(f32x4*)(red + 13 * 256 + kt2 * 16 + 4 * g) = zz;
            };
            if (wc == 0) {
                s16x8 k0 = ldf(0), k1 = ldf(1), k2 = ldf(2), v0 = ldf(5), v1 = ldf(6), v2 = ldf(7);
                pairmm(0, k0, v0); pairmm(1, k0, v1); zmm(0, k0);
                pairmm(2, k1, v0); pairmm(3, k1, v1); pairmm(4, k1, v2); zmm(1, k1);
                pairmm(5, k2, v1); pairmm(6, k2, v2);
            } else {
                s16x8 k2 = ldf(2), k3 = ldf(3), k4 = ldf(4), v2 = ldf(7), v3 = ldf(8), v4 = ldf(9);
                pairmm(7, k2, v3); zmm(2, k2);
                pairmm(8, k3, v2); pairmm(9, k3, v3); pairmm(10, k3, v4); zmm(3, k3);
                pairmm(11, k4, v3); pairmm(12, k4, v4); zmm(4, k4);
            }
        }
        __syncthreads();
        const f32x4* rb = (const f32x4*)smem;
        f32x4* dst = (f32x4*)(part + ((size_t)rt * 8 + (cg - 4)) * PSZ);
        for (int i = tid; i < PSZ / 4; i += 640)
            dst[i] = (rb[i] + rb[PSZ / 4 + i]) + (rb[2 * (PSZ / 4) + i] + rb[3 * (PSZ / 4) + i]);
    }
    STAMP(0,12);
}

__global__ __launch_bounds__(640) void k1_kernel(const u16* __restrict__ x, const u16* __restrict__ w,
                                                 u16* __restrict__ pq, float* __restrict__ part,
                                                 unsigned* __restrict__ cnt,
                                                 const u16* __restrict__ wo, u16* __restrict__ woT) {
    __shared__ __attribute__((aligned(16))) char smem[K1_NST * K1_STAGE];
    int b = blockIdx.x;
    if (b >= 192) {
        const int k0 = (b - 192) * 16, tid = threadIdx.x;
        u16* t = (u16*)smem;
#pragma unroll
        for (int j = 0; j < 2; ++j) {
            int c = j * 640 + tid, r = c / 80, cc = c % 80;
            *(u32x4*)(t + r * 648 + cc * 8) = *(const u32x4*)(wo + (size_t)(k0 + r) * D_ + cc * 8);
        }
        __syncthreads();
        u32x4 o0, o1;
#pragma unroll
        for (int j = 0; j < 4; ++j) {
            o0[j] = (unsigned)t[(2 * j) * 648 + tid] | ((unsigned)t[(2 * j + 1) * 648 + tid] << 16);
            o1[j] = (unsigned)t[(8 + 2 * j) * 648 + tid] | ((unsigned)t[(9 + 2 * j) * 648 + tid] << 16);
        }
        *(u32x4*)(woT + (size_t)tid * D_ + k0) = o0;
        *(u32x4*)(woT + (size_t)tid * D_ + k0 + 8) = o1;
        return;
    }
    if (b == 0 && threadIdx.x < 8) cnt[threadIdx.x * 64] = 0u;
    int xcd = b & 7, idx = b >> 3;
    int rt = (xcd >> 1) * 4 + (idx & 3);
    int cg = (xcd & 1) * 6 + (idx >> 2);
    if (cg < 4) k1_body<true>(x, w, pq, part, smem, rt, cg);
    else        k1_body<false>(x, w, pq, part, smem, rt, cg);
}

constexpr int OST = 648;
constexpr int K2_O_BYTES = 64 * OST * 2;
constexpr int K2_CH_BYTES = 128 * 80 * 2;
typedef __attribute__((ext_vector_type(16))) float f32x16;
__device__ __forceinline__ int wo_col(int krow, int cc) {
    int t = (cc >> 1) + 5 - ((krow >> 3) & 1);
    t = t >= 5 ? t - 5 : t;
    return t * 16 + (cc & 1) * 8;
}

__global__ __launch_bounds__(640) void k2_kernel(const u16* __restrict__ pq, const float* __restrict__ part,
                                                 u16* kvfrag, unsigned* cnt,
                                                 const u16* __restrict__ wo, u16* __restrict__ out) {
    __shared__ __attribute__((aligned(16))) char smem[K2_O_BYTES + 3 * K2_CH_BYTES];
    const int tid = threadIdx.x, lane = tid & 63;
    const int wv = __builtin_amdgcn_readfirstlane(tid >> 6);
    const int g = lane >> 4, r16 = lane & 15;
    const int b = blockIdx.x;
    const int xcd = b & 7, idx = b >> 3;
    const int rt = xcd * 4 + (idx & 3), ct = idx >> 2;
    const int row0 = rt * 64, col0 = ct * 80;
    char* oimg = smem;
    char* wbuf = smem + K2_O_BYTES;

    if (wv >= 8) {
        const int ld = wv - 8;
        if (b < 32) { __syncthreads(); __syncthreads(); __syncthreads(); }
#pragma unroll
        for (int ch = 0; ch < 3; ++ch)
#pragma unroll
            for (int i = 0; i < 10; ++i) {
                int row = (ld * 10 + i) * 4 + (lane >> 4);
                const u16* src = wo + (size_t)(col0 + row) * D_ + ch * 128 + (((lane & 15) ^ (row & 15)) * 8);
                __builtin_amdgcn_global_load_lds((const unsigned*)src,
                    (unsigned*)(wbuf + ch * K2_CH_BYTES + (ld * 10 + i) * 1024 + lane * 16), 16, 0, 0);
            }
        u32x4 hold[20];
#pragma unroll
        for (int i = 0; i < 20; ++i) {
            int row = i * 4 + (lane >> 4);
            hold[i] = *(const u32x4*)(wo + (size_t)(col0 + row) * D_ + (3 + ld) * 128 + (((lane & 15) ^ (row & 15)) * 8));
        }
        WAIT_V(0);
        BAR();
        BAR();
        BAR();
        if (ld == 0) {
#pragma unroll
            for (int i = 0; i < 20; ++i) *(u32x4*)(wbuf + i * 1024 + lane * 16) = hold[i];
        }
        WAIT_L(0);
        BAR();
        if (ld == 1) {
#pragma unroll
            for (int i = 0; i < 20; ++i) *(u32x4*)(wbuf + K2_CH_BYTES + i * 1024 + lane * 16) = hold[i];
        }
        WAIT_L(0);
        BAR();
        BAR();
        return;
    }

    STAMP(2,0);
    float psum = 0.f;
    if (b < 32 && tid < 420) {
        int grp = b >> 2, hh = b & 3;
        int pidx;
        if (tid < 400) {
            int d = hh * 20 + tid / 20, e = hh * 20 + tid % 20;
            int kt = d >> 4, vt = e >> 4;
            int pi = kt == 0 ? vt : (3 * kt - 1 + (vt - kt + 1));
            pidx = pi * 256 + (d & 15) * 16 + (e & 15);
        } else {
            pidx = 13 * 256 + hh * 20 + (tid - 400);
        }
        float v[NRT];
#pragma unroll
        for (int p = 0; p < NRT; ++p) v[p] = part[((size_t)p * 8 + grp) * PSZ + pidx];
#pragma unroll
        for (int p = 0; p < NRT; ++p) psum += v[p];
    }
    u32x4 pv[10];
#pragma unroll
    for (int j = 0; j < 10; ++j) {
        int c = j * 512 + tid;
        int row = c / 80, cc = c % 80;
        pv[j] = *(const u32x4*)(pq + (size_t)(row0 + row) * D_ + cc * 8);
    }

    if (b < 32) {
        u16* red = (u16*)wbuf;
        if (tid < 128) *(u32x4*)(red + tid * 8) = (u32x4){0u, 0u, 0u, 0u};
        __syncthreads();
        if (tid < 400) red[(tid % 20) * 32 + tid / 20] = f2bf(psum);
        else if (tid < 420) red[20 * 32 + (tid - 400)] = f2bf(psum);
        __syncthreads();
        if (tid < 128) {
            int ks = tid >> 6;
            u32x4 ov = *(const u32x4*)(red + (lane & 31) * 32 + 16 * ks + 8 * (lane >> 5));
            unsigned long long* dst = (unsigned long long*)(kvfrag + ((size_t)(b * 2 + ks) * 64 + lane) * 8);
            __hip_atomic_store(dst, (unsigned long long)ov[0] | ((unsigned long long)ov[1] << 32), __ATOMIC_RELAXED, __HIP_MEMORY_SCOPE_AGENT);
            __hip_atomic_store(dst + 1, (unsigned long long)ov[2] | ((unsigned long long)ov[3] << 32), __ATOMIC_RELAXED, __HIP_MEMORY_SCOPE_AGENT);
        }
        WAIT_V(0);
        __syncthreads();
        if (tid < 8) __hip_atomic_fetch_add(cnt + tid * 64, 1u, __ATOMIC_RELAXED, __HIP_MEMORY_SCOPE_AGENT);
        STAMP(1,0);
    }

#pragma unroll
    for (int j = 0; j < 10; ++j) {
        int c = j * 512 + tid;
        int row = c / 80, cc = c % 80;
        *(u32x4*)(oimg + row * (OST * 2) + cc * 16) = pv[j];
    }
    if (tid < 64) *(u32x4*)(oimg + tid * (OST * 2) + 1280) = (u32x4){0u, 0u, 0u, 0u};

    if (tid == 0) {
        unsigned spins = 0;
        while (__hip_atomic_load(cnt + (b & 7) * 64, __ATOMIC_RELAXED, __HIP_MEMORY_SCOPE_AGENT) < 32u) {
            __builtin_amdgcn_s_sleep(4);
            if (++spins > (1u << 24)) break;
        }
        __builtin_amdgcn_fence(__ATOMIC_ACQUIRE, "wavefront");
    }
    WAIT_L(0);
    BAR();
    STAMP(2,8);

    s16x8 a0[4], a1[4];
    {
        const int row = lane & 31;
#pragma unroll
        for (int hh = 0; hh < 4; ++hh) {
            int h = wv * 4 + hh;
            u32x4 v0 = (u32x4){0u, 0u, 0u, 0u}, v1 = (u32x4){0u, 0u, 0u, 0u};
            if (row <= 20) {
                const unsigned long long* s0 = (const unsigned long long*)(kvfrag + ((size_t)(h * 2 + 0) * 64 + lane) * 8);
                unsigned long long t0 = __hip_atomic_load(s0, __ATOMIC_RELAXED, __HIP_MEMORY_SCOPE_AGENT);
                unsigned long long t1 = __hip_atomic_load(s0 + 1, __ATOMIC_RELAXED, __HIP_MEMORY_SCOPE_AGENT);
                v0 = (u32x4){(unsigned)t0, (unsigned)(t0 >> 32), (unsigned)t1, (unsigned)(t1 >> 32)};
                if (lane < 32) {
                    const unsigned long long* s1 = (const unsigned long long*)(kvfrag + ((size_t)(h * 2 + 1) * 64 + lane) * 8);
                    unsigned long long t2 = __hip_atomic_load(s1, __ATOMIC_RELAXED, __HIP_MEMORY_SCOPE_AGENT);
                    unsigned long long t3 = __hip_atomic_load(s1 + 1, __ATOMIC_RELAXED, __HIP_MEMORY_SCOPE_AGENT);
                    v1 = (u32x4){(unsigned)t2, (unsigned)(t2 >> 32), (unsigned)t3, (unsigned)(t3 >> 32)};
                }
            }
            a0[hh] = __builtin_bit_cast(s16x8, v0);
            a1[hh] = __builtin_bit_cast(s16x8, v1);
        }
    }
    STAMP(2,9);
    {
        const int hl = lane >> 5;
        char* rbase = oimg + (lane & 31) * (OST * 2) + wv * 160 + hl * 16;
        char* wbase = oimg + (lane & 31) * (OST * 2) + wv * 160 + hl * 8;
        f32x16 zero16;
#pragma unroll
        for (int i = 0; i < 16; ++i) zero16[i] = 0.f;
        u32x4 b0[8], b1[8];
#pragma unroll
        for (int u = 0; u < 8; ++u) {
            const char* rp = rbase + (u & 1) * 32 * (OST * 2) + (u >> 1) * 40;
            u32x2 l0 = *(const u32x2*)(rp), l1 = *(const u32x2*)(rp + 8);
            u32x2 l2 = *(const u32x2*)(rp + 32), l3 = *(const u32x2*)(rp + 40);
            b0[u] = (u32x4){l0[0], l0[1], l1[0], l1[1]};
            b1[u] = (u32x4){l2[0], l2[1], l3[0], l3[1]};
        }
#pragma unroll
        for (int half = 0; half < 2; ++half) {
            f32x16 dd[4];
#pragma unroll
            for (int v = 0; v < 4; ++v) {
                const int u = half * 4 + v;
                dd[v] = __builtin_amdgcn_mfma_f32_32x32x16_bf16(a0[u >> 1], __builtin_bit_cast(s16x8, b0[u]), zero16, 0, 0, 0);
            }
#pragma unroll
            for (int v = 0; v < 4; ++v) {
                const int u = half * 4 + v;
                dd[v] = __builtin_amdgcn_mfma_f32_32x32x16_bf16(a1[u >> 1], __builtin_bit_cast(s16x8, b1[u]), dd[v], 0, 0, 0);
            }
            __builtin_amdgcn_sched_barrier(0);
#pragma unroll
            for (int v = 0; v < 4; ++v) {
                const int u = half * 4 + v;
                unsigned du = __float_as_uint(dd[v][8]);
                auto sw = __builtin_amdgcn_permlane32_swap(du, du, false, false);
                float den = __uint_as_float(sw[1]);
                den = rbf(rbf(den) + 1e-6f);
                float inv = __builtin_amdgcn_rcpf(den);
                char* wp = wbase + (u & 1) * 32 * (OST * 2) + (u >> 1) * 40;
#pragma unroll
                for (int q = 0; q < 3; ++q) {
                    u32x2 vv;
                    unsigned n0 = pack2(dd[v][4 * q + 0], dd[v][4 * q + 1]), n1 = pack2(dd[v][4 * q + 2], dd[v][4 * q + 3]);
                    vv[0] = pack2(lo2f(n0) * inv, hi2f(n0) * inv);
                    vv[1] = pack2(lo2f(n1) * inv, hi2f(n1) * inv);
                    if (q < 2) *(u32x2*)(wp + q * 16) = vv;
                    else if (hl == 0) *(u32x2*)(wp + 32) = vv;
                }
            }
        }
    }
    STAMP(2,1);

    const int wr = wv & 3, wc = wv >> 2;
    const f32x4 zero4 = (f32x4){0.f, 0.f, 0.f, 0.f};
    f32x4 acc[3];
#pragma unroll
    for (int n = 0; n < 3; ++n) acc[n] = zero4;
    int w_off[4];
#pragma unroll
    for (int ks = 0; ks < 4; ++ks) w_off[ks] = (wc * 48 + r16) * 256 + (((4 * ks + g) ^ r16) * 16);
    const int orow = wr * 16 + r16;
    const char* obase = oimg + orow * (OST * 2) + g * 16;

    auto phase_b = [&](auto NT) {
        constexpr int nt = decltype(NT)::value;
#pragma unroll 1
        for (int ch = 0; ch < 5; ++ch) {
            WAIT_L(0);
            BAR();
            STAMP(2,2+ch);
            const char* wb = wbuf + (ch >= 3 ? ch - 3 : ch) * K2_CH_BYTES;
            const char* ob = obase + ch * 256;
            s16x8 of[4], wf[4][nt];
#pragma unroll
            for (int ks = 0; ks < 4; ++ks) {
                of[ks] = *(const s16x8*)(ob + ks * 64);
#pragma unroll
                for (int n = 0; n < nt; ++n) {
                    wf[ks][n] = *(const s16x8*)(wb + w_off[ks] + n * 4096);
                }
            }
#pragma unroll
            for (int ks = 0; ks < 4; ++ks)
#pragma unroll
                for (int n = 0; n < nt; ++n)
                    acc[n] = __builtin_amdgcn_mfma_f32_16x16x32_bf16(wf[ks][n], of[ks], acc[n], 0, 0, 0);
        }
    };
    if (wc == 0) phase_b(std::integral_constant<int, 3>{});
    else         phase_b(std::integral_constant<int, 2>{});
    STAMP(2,7);
    int s = row0 + orow;
#pragma unroll
    for (int n = 0; n < 3; ++n) {
        if (n == 2 && wc == 1) continue;
        u32x2 v;
        v[0] = pack2(acc[n][0], acc[n][1]);
        v[1] = pack2(acc[n][2], acc[n][3]);
        *(u32x2*)(out + (size_t)s * D_ + col0 + (wc * 3 + n) * 16 + 4 * g) = v;
    }
}

extern "C" void kernel_launch(void* const* d_in, const int* in_sizes, int n_in,
                              void* d_out, int out_size, void* d_ws, size_t ws_size,
                              hipStream_t stream) {
    const u16* x = (const u16*)d_in[0];
    const u16* wqkv = (const u16*)d_in[1];
    const u16* wo = (const u16*)d_in[2];
    u16* out = (u16*)d_out;
    char* ws = (char*)d_ws;
    u16* pq = (u16*)ws;
    float* part = (float*)(ws + 4u * 1024 * 1024);
    u16* kvfrag = (u16*)(ws + 7u * 1024 * 1024);
    unsigned* cnt = (unsigned*)(ws + 8u * 1024 * 1024);
    u16* woT = (u16*)(ws + 10u * 1024 * 1024);
    k1_kernel<<<232, 640, 0, stream>>>(x, wqkv, pq, part, cnt, wo, woT);
    k2_kernel<<<256, 640, 0, stream>>>(pq, part, kvfrag, cnt, woT, out);
}
```

```cpp
#include <hip/hip_runtime.h>
#include <hip/hip_bf16.h>
#include <stdint.h>
#include <type_traits>

typedef unsigned short u16;
typedef __attribute__((ext_vector_type(8))) short s16x8;
typedef __attribute__((ext_vector_type(4))) short s16x4;
typedef __attribute__((ext_vector_type(4))) float f32x4;
typedef __attribute__((ext_vector_type(2))) unsigned int u32x2;
typedef __attribute__((ext_vector_type(4))) unsigned int u32x4;

constexpr int S_ = 2048, D_ = 640, H_ = 32, DH_ = 20, N3_ = 1920;
constexpr int NRT = 16;
constexpr int PSZ = 13 * 256 + 80;

#define LDS_AS __attribute__((address_space(3)))

#define STAMP(k,i)

__device__ __forceinline__ float bf2f(u16 u) { return __uint_as_float(((unsigned)u) << 16); }
__device__ __forceinline__ u16 f2bf(float f) { __bf16 b = (__bf16)f; return __builtin_bit_cast(u16, b); }
__device__ __forceinline__ float rbf(float f) { return bf2f(f2bf(f)); }
typedef __attribute__((ext_vector_type(2))) float f32x2;
typedef __attribute__((ext_vector_type(2))) __bf16 bf16x2;
__device__ __forceinline__ unsigned pack2(float a, float b) {
    f32x2 v = {a, b};
    return __builtin_bit_cast(unsigned, __builtin_convertvector(v, bf16x2));
}
__device__ __forceinline__ float lo2f(unsigned u) { return __uint_as_float(u << 16); }
__device__ __forceinline__ float hi2f(unsigned u) { return __uint_as_float(u & 0xffff0000u); }
__device__ __forceinline__ unsigned phi2(float a, float b) {
    unsigned t = pack2(a, b);
    float ta = lo2f(t), tb = hi2f(t);
    float pa = ta > 0.f ? ta + 1.0f : __expf(ta);
    float pb = tb > 0.f ? tb + 1.0f : __expf(tb);
    unsigned p = pack2(pa, pb);
    return pack2(lo2f(p) + 1e-6f, hi2f(p) + 1e-6f);
}
__device__ __forceinline__ float phi(float acc) {
    float t = rbf(acc);
    float p = t > 0.f ? t + 1.0f : __expf(t);
    return rbf(rbf(p) + 1e-6f);
}

#define WAIT_V(n) asm volatile("s_waitcnt vmcnt(" #n ")" ::: "memory")
#define WAIT_L(n) asm volatile("s_waitcnt lgkmcnt(" #n ")" ::: "memory")
#define BAR() __builtin_amdgcn_s_barrier()

__device__ __forceinline__ s16x4 tr_read(const char* lds_ptr) {
    return __builtin_amdgcn_ds_read_tr16_b64_v4i16((LDS_AS s16x4*)lds_ptr);
}
__device__ __forceinline__ s16x8 cat8(s16x4 a, s16x4 b) {
    s16x8 r; r[0]=a[0]; r[1]=a[1]; r[2]=a[2]; r[3]=a[3]; r[4]=b[0]; r[5]=b[1]; r[6]=b[2]; r[7]=b[3]; return r;
}

constexpr int K1_A_BYTES = 128 * 64 * 2;
constexpr int K1_B_BYTES = 64 * 160 * 2;
constexpr int K1_STAGE = K1_A_BYTES + K1_B_BYTES;
constexpr int K1_NST = 4;

template <bool IS_Q>
__device__ __forceinline__ void k1_body(const u16* __restrict__ x, const u16* __restrict__ w,
                                        u16* __restrict__ pq, float* __restrict__ part,
                                        char* smem, int rt, int cg) {
    const int tid = threadIdx.x, lane = tid & 63;
    const int wv = __builtin_amdgcn_readfirstlane(tid >> 6);
    const int wr = wv & 3, wc = (wv >> 2) & 1;
    const int g = lane >> 4, r16 = lane & 15;
    const int row0 = rt * 128;

    f32x4 acc[2][5];
#pragma unroll
    for (int m = 0; m < 2; ++m)
#pragma unroll
        for (int n = 0; n < 5; ++n) acc[m][n] = (f32x4){0.f, 0.f, 0.f, 0.f};

    STAMP(0,0);
    if (wv >= 8) {
        const int ld = wv - 8;
        int a_src[8], b_src[10];
#pragma unroll
        for (int i = 0; i < 8; ++i) {
            int row = (ld * 8 + i) * 8 + (lane >> 3), slot = lane & 7;
            a_src[i] = (row0 + row) * D_ + ((slot ^ (row & 7)) * 8);
        }
#pragma unroll
        for (int i = 0; i < 8; ++i)
            __builtin_amdgcn_global_load_lds((const unsigned*)(x + a_src[i]), (unsigned*)(smem + ld * 8192 + lane * 16 + i * 1024), 16, 0, 0);
        __builtin_amdgcn_sched_barrier(0);
#pragma unroll
        for (int i = 0; i < 10; ++i) {
            int c = (ld * 10 + i) * 64 + lane;
            int krow = c / 20, cc = c % 20;
            int sc = cc ^ (((krow >> 3) & 1) << 1);
            int col = sc * 8;
            int gcol;
            if (IS_Q) gcol = cg * 160 + col;
            else gcol = (col < 80) ? (640 + (cg - 4) * 80 + col) : (1280 + (cg - 4) * 80 + (col - 80));
            b_src[i] = krow * N3_ + gcol;
        }
        auto stage = [&](int st, int kt) {
            char* a_dst = smem + st * K1_STAGE + ld * 8192 + lane * 16;
            char* b_dst = smem + st * K1_STAGE + K1_A_BYTES + ld * 10240 + lane * 16;
#pragma unroll
            for (int i = 0; i < 8; ++i)
                __builtin_amdgcn_global_load_lds((const unsigned*)(x + a_src[i] + kt * 64), (unsigned*)(a_dst + i * 1024), 16, 0, 0);
#pragma unroll
            for (int i = 0; i < 10; ++i)
                __builtin_amdgcn_global_load_lds((const unsigned*)(w + b_src[i] + kt * 64 * N3_), (unsigned*)(b_dst + i * 1024), 16, 0, 0);
        };
#pragma unroll
        for (int i = 0; i < 10; ++i)
            __builtin_amdgcn_global_load_lds((const unsigned*)(w + b_src[i]), (unsigned*)(smem + K1_A_BYTES + ld * 10240 + lane * 16 + i * 1024), 16, 0, 0);
        stage(1, 1); stage(2, 2);
        WAIT_V(36);
        BAR();
#pragma unroll 1
        for (int kt = 0; kt < 10; ++kt) {
            if (kt < 7) WAIT_V(18); else WAIT_V(0);
            BAR();
            if (kt + 3 < 10) stage((kt + 3) & 3, kt + 3);
        }
    } else {
        int a_off[2][2];
#pragma unroll
        for (int m = 0; m < 2; ++m)
#pragma unroll
            for (int ks = 0; ks < 2; ++ks) {
                int row = wr * 32 + m * 16 + r16;
                a_off[m][ks] = row * 128 + (((4 * ks + g) ^ (row & 7)) * 16);
            }
        const int q4 = (lane & 15) >> 2, p4 = lane & 3;
        int b_off[5];
#pragma unroll
        for (int n = 0; n < 5; ++n) {
            int tl = IS_Q ? wc * 5 + n : (wc == 0 ? (n < 3 ? n : n + 2) : (n < 2 ? n + 3 : n + 5));
            b_off[n] = (8 * g + q4) * 320 + p4 * 8 + ((tl ^ (g & 1)) * 32);
        }

        s16x8 af0[2], bq0[5], af1[2], bq1[5];
        auto rd = [&](s16x8* af, s16x8* bq, int kt, int ks) {
            const char* a_base = smem + (kt & 3) * K1_STAGE;
            const char* b_base = a_base + K1_A_BYTES;
#pragma unroll
            for (int m = 0; m < 2; ++m) af[m] = *(const s16x8*)(a_base + a_off[m][ks]);
#pragma unroll
            for (int n = 0; n < 5; ++n) {
                const char* p = b_base + b_off[n] + (32 * ks) * 320;
                bq[n] = cat8(tr_read(p), tr_read(p + 4 * 320));
            }
        };
        auto mm = [&](const s16x8* af, const s16x8* bq) {
#pragma unroll
            for (int n = 0; n < 5; ++n) {
#pragma unroll
                for (int m = 0; m < 2; ++m) {
                    if (IS_Q) acc[m][n] = __builtin_amdgcn_mfma_f32_16x16x32_bf16(bq[n], af[m], acc[m][n], 0, 0, 0);
                    else      acc[m][n] = __builtin_amdgcn_mfma_f32_16x16x32_bf16(af[m], bq[n], acc[m][n], 0, 0, 0);
                }
            }
        };
        BAR();
        rd(af0, bq0, 0, 0);
#pragma unroll 1
        for (int kt = 0; kt < 10; ++kt) {
            BAR();
            STAMP(0,1+kt);
            __builtin_amdgcn_s_waitcnt(0xC07F);
            __builtin_amdgcn_sched_barrier(0);
            rd(af1, bq1, kt, 1);
            __builtin_amdgcn_sched_barrier(0);
            mm(af0, bq0);
            __builtin_amdgcn_sched_barrier(0);
            rd(af0, bq0, kt + 1, 0);
            __builtin_amdgcn_sched_barrier(0);
            mm(af1, bq1);
            __builtin_amdgcn_sched_barrier(0);
        }
    }
    STAMP(0,11);

    if (IS_Q) {
        if (wv >= 8) return;
#pragma unroll
        for (int m = 0; m < 2; ++m) {
            int s = row0 + wr * 32 + m * 16 + r16;
#pragma unroll
            for (int n = 0; n < 5; ++n) {
                u32x2 v;
                v[0] = phi2(acc[m][n][0], acc[m][n][1]);
                v[1] = phi2(acc[m][n][2], acc[m][n][3]);
                *(u32x2*)(pq + (size_t)s * D_ + cg * 160 + wc * 80 + n * 16 + 4 * g) = v;
            }
        }
    } else {
        WAIT_L(0);
        BAR();
        char* xch = smem + 64 * 1024;
        if (wv < 8) {
            const int nk = wc == 0 ? 3 : 2;
#pragma unroll
            for (int n = 0; n < 5; ++n) {
                u32x4 fr;
                if (n < 2 || (n == 2 && nk == 3)) {
                    fr[0] = phi2(acc[0][n][0], acc[0][n][1]); fr[1] = phi2(acc[0][n][2], acc[0][n][3]);
                    fr[2] = phi2(acc[1][n][0], acc[1][n][1]); fr[3] = phi2(acc[1][n][2], acc[1][n][3]);
                } else {
                    fr[0] = pack2(acc[0][n][0], acc[0][n][1]); fr[1] = pack2(acc[0][n][2], acc[0][n][3]);
                    fr[2] = pack2(acc[1][n][0], acc[1][n][1]); fr[3] = pack2(acc[1][n][2], acc[1][n][3]);
                }
                int tl = wc == 0 ? (n < 3 ? n : n + 2) : (n < 2 ? n + 3 : n + 5);
                *(u32x4*)(xch + ((wr * 10 + tl) * 64 + lane) * 16) = fr;
            }
        }
        __syncthreads();
        if (wv < 8) {
            s16x8 ones;
#pragma unroll
            for (int j = 0; j < 8; ++j) ones[j] = (short)0x3F80;
            float* red = (float*)smem + wr * PSZ;
            const f32x4 zero4 = (f32x4){0.f, 0.f, 0.f, 0.f};
            const char* xw = xch + (wr * 10 * 64 + lane) * 16;
            auto ldf = [&](int t) { return *(const s16x8*)(xw + t * 1024); };
            auto pairmm = [&](int pi, s16x8 kf, s16x8 vfr) {
                f32x4 dd = __builtin_amdgcn_mfma_f32_16x16x32_bf16(vfr, kf, zero4, 0, 0, 0);
                *(f32x4*)(red + pi * 256 + r16 * 16 + 4 * g) = dd;
            };
            auto zmm = [&](int kt2, s16x8 kf) {
                f32x4 zz = __builtin_amdgcn_mfma_f32_16x16x32_bf16(kf, ones, zero4, 0, 0, 0);
                if (r16 == 0) *(f32x4*)(red + 13 * 256 + kt2 * 16 + 4 * g) = zz;
            };
            if (wc == 0) {
                s16x8 k0 = ldf(0), k1 = ldf(1), k2 = ldf(2), v0 = ldf(5), v1 = ldf(6), v2 = ldf(7);
                pairmm(0, k0, v0); pairmm(1, k0, v1); zmm(0, k0);
                pairmm(2, k1, v0); pairmm(3, k1, v1); pairmm(4, k1, v2); zmm(1, k1);
                pairmm(5, k2, v1); pairmm(6, k2, v2);
            } else {
                s16x8 k2 = ldf(2), k3 = ldf(3), k4 = ldf(4), v2 = ldf(7), v3 = ldf(8), v4 = ldf(9);
                pairmm(7, k2, v3); zmm(2, k2);
                pairmm(8, k3, v2); pairmm(9, k3, v3); pairmm(10, k3, v4); zmm(3, k3);
                pairmm(11, k4, v3); pairmm(12, k4, v4); zmm(4, k4);
            }
        }
        __syncthreads();
        const f32x4* rb = (const f32x4*)smem;
        f32x4* dst = (f32x4*)(part + ((size_t)rt * 8 + (cg - 4)) * PSZ);
        for (int i = tid; i < PSZ / 4; i += 640)
            dst[i] = (rb[i] + rb[PSZ / 4 + i]) + (rb[2 * (PSZ / 4) + i] + rb[3 * (PSZ / 4) + i]);
    }
    STAMP(0,12);
}

__global__ __launch_bounds__(640) void k1_kernel(const u16* __restrict__ x, const u16* __restrict__ w,
                                                 u16* __restrict__ pq, float* __restrict__ part,
                                                 unsigned* __restrict__ cnt,
                                                 const u16* __restrict__ wo, u16* __restrict__ woT) {
    __shared__ __attribute__((aligned(16))) char smem[K1_NST * K1_STAGE];
    int b = blockIdx.x;
    if (b >= 192) {
        const int k0 = (b - 192) * 16, tid = threadIdx.x;
        u16* t = (u16*)smem;
#pragma unroll
        for (int j = 0; j < 2; ++j) {
            int c = j * 640 + tid, r = c / 80, cc = c % 80;
            *(u32x4*)(t + r * 648 + cc * 8) = *(const u32x4*)(wo + (size_t)(k0 + r) * D_ + cc * 8);
        }
        __syncthreads();
        u32x4 o0, o1;
#pragma unroll
        for (int j = 0; j < 4; ++j) {
            o0[j] = (unsigned)t[(2 * j) * 648 + tid] | ((unsigned)t[(2 * j + 1) * 648 + tid] << 16);
            o1[j] = (unsigned)t[(8 + 2 * j) * 648 + tid] | ((unsigned)t[(9 + 2 * j) * 648 + tid] << 16);
        }
        *(u32x4*)(woT + (size_t)tid * D_ + k0) = o0;
        *(u32x4*)(woT + (size_t)tid * D_ + k0 + 8) = o1;
        return;
    }
    if (b == 0 && threadIdx.x < 8) cnt[threadIdx.x * 64] = 0u;
    int xcd = b & 7, idx = b >> 3;
    int rt = (xcd >> 1) * 4 + (idx & 3);
    int cg = (xcd & 1) * 6 + (idx >> 2);
    if (cg < 4) k1_body<true>(x, w, pq, part, smem, rt, cg);
    else        k1_body<false>(x, w, pq, part, smem, rt, cg);
}

constexpr int OST = 648;
constexpr int K2_O_BYTES = 64 * OST * 2;
constexpr int K2_CH_BYTES = 128 * 80 * 2;
typedef __attribute__((ext_vector_type(16))) float f32x16;
__device__ __forceinline__ int wo_col(int krow, int cc) {
    int t = (cc >> 1) + 5 - ((krow >> 3) & 1);
    t = t >= 5 ? t - 5 : t;
    return t * 16 + (cc & 1) * 8;
}

__global__ __launch_bounds__(640) void k2_kernel(const u16* __restrict__ pq, const float* __restrict__ part,
                                                 u16* kvfrag, unsigned* cnt,
                                                 const u16* __restrict__ wo, u16* __restrict__ out) {
    __shared__ __attribute__((aligned(16))) char smem[K2_O_BYTES + 3 * K2_CH_BYTES];
    const int tid = threadIdx.x, lane = tid & 63;
    const int wv = __builtin_amdgcn_readfirstlane(tid >> 6);
    const int g = lane >> 4, r16 = lane & 15;
    const int b = blockIdx.x;
    const int xcd = b & 7, idx = b >> 3;
    const int rt = xcd * 4 + (idx & 3), ct = idx >> 2;
    const int row0 = rt * 64, col0 = ct * 80;
    char* oimg = smem;
    char* wbuf = smem + K2_O_BYTES;

    if (wv >= 8) {
        const int ld = wv - 8;
        if (b < 32) { __syncthreads(); __syncthreads(); __syncthreads(); }
#pragma unroll
        for (int ch = 0; ch < 3; ++ch)
#pragma unroll
            for (int i = 0; i < 10; ++i) {
                int row = (ld * 10 + i) * 4 + (lane >> 4);
                const u16* src = wo + (size_t)(col0 + row) * D_ + ch * 128 + (((lane & 15) ^ (row & 15)) * 8);
                __builtin_amdgcn_global_load_lds((const unsigned*)src,
                    (unsigned*)(wbuf + ch * K2_CH_BYTES + (ld * 10 + i) * 1024 + lane * 16), 16, 0, 0);
            }
        u32x4 hold[20];
#pragma unroll
        for (int i = 0; i < 20; ++i) {
            int row = i * 4 + (lane >> 4);
            hold[i] = *(const u32x4*)(wo + (size_t)(col0 + row) * D_ + (3 + ld) * 128 + (((lane & 15) ^ (row & 15)) * 8));
        }
        WAIT_V(0);
        BAR();
        BAR();
        BAR();
        if (ld == 0) {
#pragma unroll
            for (int i = 0; i < 20; ++i) *(u32x4*)(wbuf + i * 1024 + lane * 16) = hold[i];
        }
        WAIT_L(0);
        BAR();
        if (ld == 1) {
#pragma unroll
            for (int i = 0; i < 20; ++i) *(u32x4*)(wbuf + K2_CH_BYTES + i * 1024 + lane * 16) = hold[i];
        }
        WAIT_L(0);
        BAR();
        BAR();
        return;
    }

    STAMP(2,0);
    float psum = 0.f;
    if (b < 32 && tid < 420) {
        int grp = b >> 2, hh = b & 3;
        int pidx;
        if (tid < 400) {
            int d = hh * 20 + tid / 20, e = hh * 20 + tid % 20;
            int kt = d >> 4, vt = e >> 4;
            int pi = kt == 0 ? vt : (3 * kt - 1 + (vt - kt + 1));
            pidx = pi * 256 + (d & 15) * 16 + (e & 15);
        } else {
            pidx = 13 * 256 + hh * 20 + (tid - 400);
        }
        float v[NRT];
#pragma unroll
        for (int p = 0; p < NRT; ++p) v[p] = part[((size_t)p * 8 + grp) * PSZ + pidx];
#pragma unroll
        for (int p = 0; p < NRT; ++p) psum += v[p];
    }
    u32x4 pv[10];
#pragma unroll
    for (int j = 0; j < 10; ++j) {
        int c = j * 512 + tid;
        int row = c / 80, cc = c % 80;
        pv[j] = *(const u32x4*)(pq + (size_t)(row0 + row) * D_ + cc * 8);
    }

    if (b < 32) {
        u16* red = (u16*)wbuf;
        if (tid < 128) *(u32x4*)(red + tid * 8) = (u32x4){0u, 0u, 0u, 0u};
        __syncthreads();
        if (tid < 400) red[(tid % 20) * 32 + tid / 20] = f2bf(psum);
        else if (tid < 420) red[20 * 32 + (tid - 400)] = f2bf(psum);
        __syncthreads();
        if (tid < 128) {
            int ks = tid >> 6;
            u32x4 ov = *(const u32x4*)(red + (lane & 31) * 32 + 16 * ks + 8 * (lane >> 5));
            unsigned long long* dst = (unsigned long long*)(kvfrag + ((size_t)(b * 2 + ks) * 64 + lane) * 8);
            __hip_atomic_store(dst, (unsigned long long)ov[0] | ((unsigned long long)ov[1] << 32), __ATOMIC_RELAXED, __HIP_MEMORY_SCOPE_AGENT);
            __hip_atomic_store(dst + 1, (unsigned long long)ov[2] | ((unsigned long long)ov[3] << 32), __ATOMIC_RELAXED, __HIP_MEMORY_SCOPE_AGENT);
        }
        WAIT_V(0);
        __syncthreads();
        if (tid < 8) __hip_atomic_fetch_add(cnt + tid * 64, 1u, __ATOMIC_RELAXED, __HIP_MEMORY_SCOPE_AGENT);
        STAMP(1,0);
    }

#pragma unroll
    for (int j = 0; j < 10; ++j) {
        int c = j * 512 + tid;
        int row = c / 80, cc = c % 80;
        *(u32x4*)(oimg + row * (OST * 2) + cc * 16) = pv[j];
    }
    if (tid < 64) *(u32x4*)(oimg + tid * (OST * 2) + 1280) = (u32x4){0u, 0u, 0u, 0u};

    if (tid == 0) {
        unsigned spins = 0;
        while (__hip_atomic_load(cnt + (b & 7) * 64, __ATOMIC_RELAXED, __HIP_MEMORY_SCOPE_AGENT) < 32u) {
            __builtin_amdgcn_s_sleep(4);
            if (++spins > (1u << 24)) break;
        }
        __builtin_amdgcn_fence(__ATOMIC_ACQUIRE, "wavefront");
    }
    WAIT_L(0);
    BAR();
    STAMP(2,8);

    s16x8 a0[4], a1[4];
    {
        const int row = lane & 31;
#pragma unroll
        for (int hh = 0; hh < 4; ++hh) {
            int h = wv * 4 + hh;
            u32x4 v0 = (u32x4){0u, 0u, 0u, 0u}, v1 = (u32x4){0u, 0u, 0u, 0u};
            if (row <= 20) {
                const unsigned long long* s0 = (const unsigned long long*)(kvfrag + ((size_t)(h * 2 + 0) * 64 + lane) * 8);
                unsigned long long t0 = __hip_atomic_load(s0, __ATOMIC_RELAXED, __HIP_MEMORY_SCOPE_AGENT);
                unsigned long long t1 = __hip_atomic_load(s0 + 1, __ATOMIC_RELAXED, __HIP_MEMORY_SCOPE_AGENT);
                v0 = (u32x4){(unsigned)t0, (unsigned)(t0 >> 32), (unsigned)t1, (unsigned)(t1 >> 32)};
                if (lane < 32) {
                    const unsigned long long* s1 = (const unsigned long long*)(kvfrag + ((size_t)(h * 2 + 1) * 64 + lane) * 8);
                    unsigned long long t2 = __hip_atomic_load(s1, __ATOMIC_RELAXED, __HIP_MEMORY_SCOPE_AGENT);
                    unsigned long long t3 = __hip_atomic_load(s1 + 1, __ATOMIC_RELAXED, __HIP_MEMORY_SCOPE_AGENT);
                    v1 = (u32x4){(unsigned)t2, (unsigned)(t2 >> 32), (unsigned)t3, (unsigned)(t3 >> 32)};
                }
            }
            a0[hh] = __builtin_bit_cast(s16x8, v0);
            a1[hh] = __builtin_bit_cast(s16x8, v1);
        }
    }
    STAMP(2,9);
    {
        const int hl = lane >> 5;
        char* rbase = oimg + (lane & 31) * (OST * 2) + wv * 160 + hl * 16;
        char* wbase = oimg + (lane & 31) * (OST * 2) + wv * 160 + hl * 8;
        f32x16 zero16;
#pragma unroll
        for (int i = 0; i < 16; ++i) zero16[i] = 0.f;
        u32x4 b0[8], b1[8];
#pragma unroll
        for (int u = 0; u < 8; ++u) {
            const char* rp = rbase + (u & 1) * 32 * (OST * 2) + (u >> 1) * 40;
            u32x2 l0 = *(const u32x2*)(rp), l1 = *(const u32x2*)(rp + 8);
            u32x2 l2 = *(const u32x2*)(rp + 32), l3 = *(const u32x2*)(rp + 40);
            b0[u] = (u32x4){l0[0], l0[1], l1[0], l1[1]};
            b1[u] = (u32x4){l2[0], l2[1], l3[0], l3[1]};
        }
#pragma unroll
        for (int half = 0; half < 2; ++half) {
            f32x16 dd[4];
#pragma unroll
            for (int v = 0; v < 4; ++v) {
                const int u = half * 4 + v;
                dd[v] = __builtin_amdgcn_mfma_f32_32x32x16_bf16(a0[u >> 1], __builtin_bit_cast(s16x8, b0[u]), zero16, 0, 0, 0);
            }
#pragma unroll
            for (int v = 0; v < 4; ++v) {
                const int u = half * 4 + v;
                dd[v] = __builtin_amdgcn_mfma_f32_32x32x16_bf16(a1[u >> 1], __builtin_bit_cast(s16x8, b1[u]), dd[v], 0, 0, 0);
            }
            __builtin_amdgcn_sched_barrier(0);
#pragma unroll
            for (int v = 0; v < 4; ++v) {
                const int u = half * 4 + v;
                unsigned du = __float_as_uint(dd[v][8]);
                auto sw = __builtin_amdgcn_permlane32_swap(du, du, false, false);
                float den = __uint_as_float(sw[1]);
                den = rbf(rbf(den) + 1e-6f);
                float inv = __builtin_amdgcn_rcpf(den);
                char* wp = wbase + (u & 1) * 32 * (OST * 2) + (u >> 1) * 40;
#pragma unroll
                for (int q = 0; q < 3; ++q) {
                    u32x2 vv;
                    unsigned n0 = pack2(dd[v][4 * q + 0], dd[v][4 * q + 1]), n1 = pack2(dd[v][4 * q + 2], dd[v][4 * q + 3]);
                    vv[0] = pack2(lo2f(n0) * inv, hi2f(n0) * inv);
                    vv[1] = pack2(lo2f(n1) * inv, hi2f(n1) * inv);
                    if (q < 2) *(u32x2*)(wp + q * 16) = vv;
                    else if (hl == 0) *(u32x2*)(wp + 32) = vv;
                }
            }
        }
    }
    STAMP(2,1);

    const int wr = wv & 3, wc = wv >> 2;
    const f32x4 zero4 = (f32x4){0.f, 0.f, 0.f, 0.f};
    f32x4 acc[3];
#pragma unroll
    for (int n = 0; n < 3; ++n) acc[n] = zero4;
    int w_off[4];
#pragma unroll
    for (int ks = 0; ks < 4; ++ks) w_off[ks] = (wc * 48 + r16) * 256 + (((4 * ks + g) ^ r16) * 16);
    const int orow = wr * 16 + r16;
    const char* obase = oimg + orow * (OST * 2) + g * 16;

    auto phase_b = [&](auto NT) {
        constexpr int nt = decltype(NT)::value;
#pragma unroll 1
        for (int ch = 0; ch < 5; ++ch) {
            WAIT_L(0);
            BAR();
            STAMP(2,2+ch);
            const char* wb = wbuf + (ch >= 3 ? ch - 3 : ch) * K2_CH_BYTES;
            const char* ob = obase + ch * 256;
            s16x8 of[4], wf[4][nt];
#pragma unroll
            for (int ks = 0; ks < 4; ++ks) {
                of[ks] = *(const s16x8*)(ob + ks * 64);
#pragma unroll
                for (int n = 0; n < nt; ++n) {
                    wf[ks][n] = *(const s16x8*)(wb + w_off[ks] + n * 4096);
                }
            }
#pragma unroll
            for (int ks = 0; ks < 4; ++ks)
#pragma unroll
                for (int n = 0; n < nt; ++n)
                    acc[n] = __builtin_amdgcn_mfma_f32_16x16x32_bf16(wf[ks][n], of[ks], acc[n], 0, 0, 0);
        }
    };
    if (wc == 0) phase_b(std::integral_constant<int, 3>{});
    else         phase_b(std::integral_constant<int, 2>{});
    STAMP(2,7);
    int s = row0 + orow;
#pragma unroll
    for (int n = 0; n < 3; ++n) {
        if (n == 2 && wc == 1) continue;
        u32x2 v;
        v[0] = pack2(acc[n][0], acc[n][1]);
        v[1] = pack2(acc[n][2], acc[n][3]);
        *(u32x2*)(out + (size_t)s * D_ + col0 + (wc * 3 + n) * 16 + 4 * g) = v;
    }
}

extern "C" void kernel_launch(void* const* d_in, const int* in_sizes, int n_in,
                              void* d_out, int out_size, void* d_ws, size_t ws_size,
                              hipStream_t stream) {
    const u16* x = (const u16*)d_in[0];
    const u16* wqkv = (const u16*)d_in[1];
    const u16* wo = (const u16*)d_in[2];
    u16* out = (u16*)d_out;
    char* ws = (char*)d_ws;
    u16* pq = (u16*)ws;
    float* part = (float*)(ws + 4u * 1024 * 1024);
    u16* kvfrag = (u16*)(ws + 7u * 1024 * 1024);
    unsigned* cnt = (unsigned*)(ws + 8u * 1024 * 1024);
    u16* woT = (u16*)(ws + 10u * 1024 * 1024);
    k1_kernel<<<232, 640, 0, stream>>>(x, wqkv, pq, part, cnt, wo, woT);
    k2_kernel<<<256, 640, 0, stream>>>(pq, part, kvfrag, cnt, woT, out);
}
```

```cpp
#include <hip/hip_runtime.h>
#include <hip/hip_bf16.h>
#include <stdint.h>
#include <type_traits>


typedef unsigned short u16;
typedef __attribute__((ext_vector_type(8))) short s16x8;
typedef __attribute__((ext_vector_type(4))) short s16x4;
typedef __attribute__((ext_vector_type(4))) float f32x4;
typedef __attribute__((ext_vector_type(2))) unsigned int u32x2;
typedef __attribute__((ext_vector_type(4))) unsigned int u32x4;

constexpr int S_ = 2048, D_ = 640, H_ = 32, DH_ = 20, N3_ = 1920;
constexpr int NRT = 16;
constexpr int PSZ = 13 * 256 + 80;

#define LDS_AS __attribute__((address_space(3)))


__device__ __forceinline__ float bf2f(u16 u) { return __uint_as_float(((unsigned)u) << 16); }
__device__ __forceinline__ u16 f2bf(float f) { __bf16 b = (__bf16)f; return __builtin_bit_cast(u16, b); }
__device__ __forceinline__ float rbf(float f) { return bf2f(f2bf(f)); }
typedef __attribute__((ext_vector_type(2))) float f32x2;
typedef __attribute__((ext_vector_type(2))) __bf16 bf16x2;
__device__ __forceinline__ unsigned pack2(float a, float b) {
    f32x2 v = {a, b};
    return __builtin_bit_cast(unsigned, __builtin_convertvector(v, bf16x2));
}
__device__ __forceinline__ float lo2f(unsigned u) { return __uint_as_float(u << 16); }
__device__ __forceinline__ float hi2f(unsigned u) { return __uint_as_float(u & 0xffff0000u); }
__device__ __forceinline__ unsigned phi2(float a, float b) {
    unsigned t = pack2(a, b);
    float ta = lo2f(t), tb = hi2f(t);
    float pa = ta > 0.f ? ta + 1.0f : __expf(ta);
    float pb = tb > 0.f ? tb + 1.0f : __expf(tb);
    unsigned p = pack2(pa, pb);
    return pack2(lo2f(p) + 1e-6f, hi2f(p) + 1e-6f);
}

#define WAIT_V(n) asm volatile("s_waitcnt vmcnt(" #n ")" ::: "memory")
#define WAIT_L(n) asm volatile("s_waitcnt lgkmcnt(" #n ")" ::: "memory")
#define BAR() __builtin_amdgcn_s_barrier()

__device__ __forceinline__ s16x4 tr_read(const char* lds_ptr) {
    return __builtin_amdgcn_ds_read_tr16_b64_v4i16((LDS_AS s16x4*)lds_ptr);
}
__device__ __forceinline__ s16x8 cat8(s16x4 a, s16x4 b) {
    s16x8 r; r[0]=a[0]; r[1]=a[1]; r[2]=a[2]; r[3]=a[3]; r[4]=b[0]; r[5]=b[1]; r[6]=b[2]; r[7]=b[3]; return r;
}

constexpr int K1_A_BYTES = 128 * 64 * 2;
constexpr int K1_B_BYTES = 64 * 160 * 2;
constexpr int K1_STAGE = K1_A_BYTES + K1_B_BYTES;
constexpr int K1_NST = 4;

template <bool IS_Q>
__device__ __forceinline__ void k1_body(const u16* __restrict__ x, const u16* __restrict__ w,
                                        u16* __restrict__ pq, float* __restrict__ part,
                                        char* smem, int rt, int cg) {
    const int tid = threadIdx.x, lane = tid & 63;
    const int wv = __builtin_amdgcn_readfirstlane(tid >> 6);
    const int wr = wv & 3, wc = (wv >> 2) & 1;
    const int g = lane >> 4, r16 = lane & 15;
    const int row0 = rt * 128;

    f32x4 acc[2][5];
#pragma unroll
    for (int m = 0; m < 2; ++m)
#pragma unroll
        for (int n = 0; n < 5; ++n) acc[m][n] = (f32x4){0.f, 0.f, 0.f, 0.f};
    if (wv >= 8) {
        const int ld = wv - 8;
        int a_src[8], b_src[10];
#pragma unroll
        for (int i = 0; i < 8; ++i) {
            int row = (ld * 8 + i) * 8 + (lane >> 3), slot = lane & 7;
            a_src[i] = (row0 + row) * D_ + ((slot ^ (row & 7)) * 8);
        }
#pragma unroll
        for (int i = 0; i < 8; ++i)
            __builtin_amdgcn_global_load_lds((const unsigned*)(x + a_src[i]), (unsigned*)(smem + ld * 8192 + lane * 16 + i * 1024), 16, 0, 0);
        __builtin_amdgcn_sched_barrier(0);
#pragma unroll
        for (int i = 0; i < 10; ++i) {
            int c = (ld * 10 + i) * 64 + lane;
            int krow = c / 20, cc = c % 20;
            int sc = cc ^ (((krow >> 3) & 1) << 1);
            int col = sc * 8;
            int gcol;
            if (IS_Q) gcol = cg * 160 + col;
            else gcol = (col < 80) ? (640 + (cg - 4) * 80 + col) : (1280 + (cg - 4) * 80 + (col - 80));
            b_src[i] = krow * N3_ + gcol;
        }
        auto stage = [&](int st, int kt) {
            char* a_dst = smem + st * K1_STAGE + ld * 8192 + lane * 16;
            char* b_dst = smem + st * K1_STAGE + K1_A_BYTES + ld * 10240 + lane * 16;
#pragma unroll
            for (int i = 0; i < 8; ++i)
                __builtin_amdgcn_global_load_lds((const unsigned*)(x + a_src[i] + kt * 64), (unsigned*)(a_dst + i * 1024), 16, 0, 0);
#pragma unroll
            for (int i = 0; i < 10; ++i)
                __builtin_amdgcn_global_load_lds((const unsigned*)(w + b_src[i] + kt * 64 * N3_), (unsigned*)(b_dst + i * 1024), 16, 0, 0);
        };
#pragma unroll
        for (int i = 0; i < 10; ++i)
            __builtin_amdgcn_global_load_lds((const unsigned*)(w + b_src[i]), (unsigned*)(smem + K1_A_BYTES + ld * 10240 + lane * 16 + i * 1024), 16, 0, 0);
        stage(1, 1); stage(2, 2);
        WAIT_V(36);
        BAR();
#pragma unroll 1
        for (int kt = 0; kt < 10; ++kt) {
            if (kt < 7) WAIT_V(18); else WAIT_V(0);
            BAR();
            if (kt + 3 < 10) stage((kt + 3) & 3, kt + 3);
        }
    } else {
        int a_off[2][2];
#pragma unroll
        for (int m = 0; m < 2; ++m)
#pragma unroll
            for (int ks = 0; ks < 2; ++ks) {
                int row = wr * 32 + m * 16 + r16;
                a_off[m][ks] = row * 128 + (((4 * ks + g) ^ (row & 7)) * 16);
            }
        const int q4 = (lane & 15) >> 2, p4 = lane & 3;
        int b_off[5];
#pragma unroll
        for (int n = 0; n < 5; ++n) {
            int tl = IS_Q ? wc * 5 + n : (wc == 0 ? (n < 3 ? n : n + 2) : (n < 2 ? n + 3 : n + 5));
            b_off[n] = (8 * g + q4) * 320 + p4 * 8 + ((tl ^ (g & 1)) * 32);
        }

        s16x8 af0[2], bq0[5], af1[2], bq1[5];
        auto rd = [&](s16x8* af, s16x8* bq, int kt, int ks) {
            const char* a_base = smem + (kt & 3) * K1_STAGE;
            const char* b_base = a_base + K1_A_BYTES;
#pragma unroll
            for (int m = 0; m < 2; ++m) af[m] = *(const s16x8*)(a_base + a_off[m][ks]);
#pragma unroll
            for (int n = 0; n < 5; ++n) {
                const char* p = b_base + b_off[n] + (32 * ks) * 320;
                bq[n] = cat8(tr_read(p), tr_read(p + 4 * 320));
            }
        };
        auto mm = [&](const s16x8* af, const s16x8* bq) {
#pragma unroll
            for (int n = 0; n < 5; ++n) {
#pragma unroll
                for (int m = 0; m < 2; ++m) {
                    if (IS_Q) acc[m][n] = __builtin_amdgcn_mfma_f32_16x16x32_bf16(bq[n], af[m], acc[m][n], 0, 0, 0);
                    else      acc[m][n] = __builtin_amdgcn_mfma_f32_16x16x32_bf16(af[m], bq[n], acc[m][n], 0, 0, 0);
                }
            }
        };
        BAR();
        rd(af0, bq0, 0, 0);
#pragma unroll 1
        for (int kt = 0; kt < 10; ++kt) {
            BAR();
            __builtin_amdgcn_s_waitcnt(0xC07F);
            __builtin_amdgcn_sched_barrier(0);
            rd(af1, bq1, kt, 1);
            __builtin_amdgcn_sched_barrier(0);
            mm(af0, bq0);
            __builtin_amdgcn_sched_barrier(0);
            rd(af0, bq0, kt + 1, 0);
            __builtin_amdgcn_sched_barrier(0);
            mm(af1, bq1);
            __builtin_amdgcn_sched_barrier(0);
        }
    }

    if (IS_Q) {
        if (wv >= 8) return;
#pragma unroll
        for (int m = 0; m < 2; ++m) {
            int s = row0 + wr * 32 + m * 16 + r16;
#pragma unroll
            for (int n = 0; n < 5; ++n) {
                u32x2 v;
                v[0] = phi2(acc[m][n][0], acc[m][n][1]);
                v[1] = phi2(acc[m][n][2], acc[m][n][3]);
                *(u32x2*)(pq + (size_t)s * D_ + cg * 160 + wc * 80 + n * 16 + 4 * g) = v;
            }
        }
    } else {
        WAIT_L(0);
        BAR();
        char* xch = smem + 64 * 1024;
        if (wv < 8) {
            const int nk = wc == 0 ? 3 : 2;
#pragma unroll
            for (int n = 0; n < 5; ++n) {
                u32x4 fr;
                if (n < 2 || (n == 2 && nk == 3)) {
                    fr[0] = phi2(acc[0][n][0], acc[0][n][1]); fr[1] = phi2(acc[0][n][2], acc[0][n][3]);
                    fr[2] = phi2(acc[1][n][0], acc[1][n][1]); fr[3] = phi2(acc[1][n][2], acc[1][n][3]);
                } else {
                    fr[0] = pack2(acc[0][n][0], acc[0][n][1]); fr[1] = pack2(acc[0][n][2], acc[0][n][3]);
                    fr[2] = pack2(acc[1][n][0], acc[1][n][1]); fr[3] = pack2(acc[1][n][2], acc[1][n][3]);
                }
                int tl = wc == 0 ? (n < 3 ? n : n + 2) : (n < 2 ? n + 3 : n + 5);
                *(u32x4*)(xch + ((wr * 10 + tl) * 64 + lane) * 16) = fr;
            }
        }
        __syncthreads();
        if (wv < 8) {
            s16x8 ones;
#pragma unroll
            for (int j = 0; j < 8; ++j) ones[j] = (short)0x3F80;
            float* red = (float*)smem + wr * PSZ;
            const f32x4 zero4 = (f32x4){0.f, 0.f, 0.f, 0.f};
            const char* xw = xch + (wr * 10 * 64 + lane) * 16;
            auto ldf = [&](int t) { return *(const s16x8*)(xw + t * 1024); };
            auto pairmm = [&](int pi, s16x8 kf, s16x8 vfr) {
                f32x4 dd = __builtin_amdgcn_mfma_f32_16x16x32_bf16(vfr, kf, zero4, 0, 0, 0);
                *(f32x4*)(red + pi * 256 + r16 * 16 + 4 * g) = dd;
            };
            auto zmm = [&](int kt2, s16x8 kf) {
                f32x4 zz = __builtin_amdgcn_mfma_f32_16x16x32_bf16(kf, ones, zero4, 0, 0, 0);
                if (r16 == 0) *(f32x4*)(red + 13 * 256 + kt2 * 16 + 4 * g) = zz;
            };
            if (wc == 0) {
                s16x8 k0 = ldf(0), k1 = ldf(1), k2 = ldf(2), v0 = ldf(5), v1 = ldf(6), v2 = ldf(7);
                pairmm(0, k0, v0); pairmm(1, k0, v1); zmm(0, k0);
                pairmm(2, k1, v0); pairmm(3, k1, v1); pairmm(4, k1, v2); zmm(1, k1);
                pairmm(5, k2, v1); pairmm(6, k2, v2);
            } else {
                s16x8 k2 = ldf(2), k3 = ldf(3), k4 = ldf(4), v2 = ldf(7), v3 = ldf(8), v4 = ldf(9);
                pairmm(7, k2, v3); zmm(2, k2);
                pairmm(8, k3, v2); pairmm(9, k3, v3); pairmm(10, k3, v4); zmm(3, k3);
                pairmm(11, k4, v3); pairmm(12, k4, v4); zmm(4, k4);
            }
        }
        __syncthreads();
        const f32x4* rb = (const f32x4*)smem;
        f32x4* dst = (f32x4*)(part + ((size_t)rt * 8 + (cg - 4)) * PSZ);
        for (int i = tid; i < PSZ / 4; i += 640)
            dst[i] = (rb[i] + rb[PSZ / 4 + i]) + (rb[2 * (PSZ / 4) + i] + rb[3 * (PSZ / 4) + i]);
    }
}

__global__ __launch_bounds__(640) void k1_kernel(const u16* __restrict__ x, const u16* __restrict__ w,
                                                 u16* __restrict__ pq, float* __restrict__ part,
                                                 unsigned* __restrict__ cnt,
                                                 const u16* __restrict__ wo, u16* __restrict__ woT) {
    __shared__ __attribute__((aligned(16))) char smem[K1_NST * K1_STAGE];
    int b = blockIdx.x;
    if (b >= 192) {
        const int k0 = (b - 192) * 16, tid = threadIdx.x;
        u16* t = (u16*)smem;
#pragma unroll
        for (int j = 0; j < 2; ++j) {
            int c = j * 640 + tid, r = c / 80, cc = c % 80;
            *(u32x4*)(t + r * 648 + cc * 8) = *(const u32x4*)(wo + (size_t)(k0 + r) * D_ + cc * 8);
        }
        __syncthreads();
        u32x4 o0, o1;
#pragma unroll
        for (int j = 0; j < 4; ++j) {
            o0[j] = (unsigned)t[(2 * j) * 648 + tid] | ((unsigned)t[(2 * j + 1) * 648 + tid] << 16);
            o1[j] = (unsigned)t[(8 + 2 * j) * 648 + tid] | ((unsigned)t[(9 + 2 * j) * 648 + tid] << 16);
        }
        *(u32x4*)(woT + (size_t)tid * D_ + k0) = o0;
        *(u32x4*)(woT + (size_t)tid * D_ + k0 + 8) = o1;
        return;
    }
    if (b == 0 && threadIdx.x < 8) cnt[threadIdx.x * 64] = 0u;
    int xcd = b & 7, idx = b >> 3;
    int rt = (xcd >> 1) * 4 + (idx & 3);
    int cg = (xcd & 1) * 6 + (idx >> 2);
    if (cg < 4) k1_body<true>(x, w, pq, part, smem, rt, cg);
    else        k1_body<false>(x, w, pq, part, smem, rt, cg);
}

constexpr int OST = 648;
constexpr int K2_O_BYTES = 64 * OST * 2;
constexpr int K2_CH_BYTES = 128 * 80 * 2;
typedef __attribute__((ext_vector_type(16))) float f32x16;

__global__ __launch_bounds__(640) void k2_kernel(const u16* __restrict__ pq, const float* __restrict__ part,
                                                 u16* kvfrag, unsigned* cnt,
                                                 const u16* __restrict__ wo, u16* __restrict__ out) {
    __shared__ __attribute__((aligned(16))) char smem[K2_O_BYTES + 3 * K2_CH_BYTES];
    const int tid = threadIdx.x, lane = tid & 63;
    const int wv = __builtin_amdgcn_readfirstlane(tid >> 6);
    const int g = lane >> 4, r16 = lane & 15;
    const int b = blockIdx.x;
    const int xcd = b & 7, idx = b >> 3;
    const int rt = xcd * 4 + (idx & 3), ct = idx >> 2;
    const int row0 = rt * 64, col0 = ct * 80;
    char* oimg = smem;
    char* wbuf = smem + K2_O_BYTES;

    if (wv >= 8) {
        const int ld = wv - 8;
        if (b < 32) { __syncthreads(); __syncthreads(); __syncthreads(); }
#pragma unroll
        for (int ch = 0; ch < 3; ++ch)
#pragma unroll
            for (int i = 0; i < 10; ++i) {
                int row = (ld * 10 + i) * 4 + (lane >> 4);
                const u16* src = wo + (size_t)(col0 + row) * D_ + ch * 128 + (((lane & 15) ^ (row & 15)) * 8);
                __builtin_amdgcn_global_load_lds((const unsigned*)src,
                    (unsigned*)(wbuf + ch * K2_CH_BYTES + (ld * 10 + i) * 1024 + lane * 16), 16, 0, 0);
            }
        u32x4 hold[20];
#pragma unroll
        for (int i = 0; i < 20; ++i) {
            int row = i * 4 + (lane >> 4);
            hold[i] = *(const u32x4*)(wo + (size_t)(col0 + row) * D_ + (3 + ld) * 128 + (((lane & 15) ^ (row & 15)) * 8));
        }
        WAIT_V(0);
        BAR();
        BAR();
        BAR();
        if (ld == 0) {
#pragma unroll
            for (int i = 0; i < 20; ++i) *(u32x4*)(wbuf + i * 1024 + lane * 16) = hold[i];
        }
        WAIT_L(0);
        BAR();
        if (ld == 1) {
#pragma unroll
            for (int i = 0; i < 20; ++i) *(u32x4*)(wbuf + K2_CH_BYTES + i * 1024 + lane * 16) = hold[i];
        }
        WAIT_L(0);
        BAR();
        BAR();
        return;
    }
    float psum = 0.f;
    if (b < 32 && tid < 420) {
        int grp = b >> 2, hh = b & 3;
        int pidx;
        if (tid < 400) {
            int d = hh * 20 + tid / 20, e = hh * 20 + tid % 20;
            int kt = d >> 4, vt = e >> 4;
            int pi = kt == 0 ? vt : (3 * kt - 1 + (vt - kt + 1));
            pidx = pi * 256 + (d & 15) * 16 + (e & 15);
        } else {
            pidx = 13 * 256 + hh * 20 + (tid - 400);
        }
        float v[NRT];
#pragma unroll
        for (int p = 0; p < NRT; ++p) v[p] = part[((size_t)p * 8 + grp) * PSZ + pidx];
#pragma unroll
        for (int p = 0; p < NRT; ++p) psum += v[p];
    }
    u32x4 pv[10];
#pragma unroll
    for (int j = 0; j < 10; ++j) {
        int c = j * 512 + tid;
        int row = c / 80, cc = c % 80;
        pv[j] = *(const u32x4*)(pq + (size_t)(row0 + row) * D_ + cc * 8);
    }

    if (b < 32) {
        u16* red = (u16*)wbuf;
        if (tid < 128) *(u32x4*)(red + tid * 8) = (u32x4){0u, 0u, 0u, 0u};
        __syncthreads();
        if (tid < 400) red[(tid % 20) * 32 + tid / 20] = f2bf(psum);
        else if (tid < 420) red[20 * 32 + (tid - 400)] = f2bf(psum);
        __syncthreads();
        if (tid < 128) {
            int ks = tid >> 6;
            u32x4 ov = *(const u32x4*)(red + (lane & 31) * 32 + 16 * ks + 8 * (lane >> 5));
            unsigned long long* dst = (unsigned long long*)(kvfrag + ((size_t)(b * 2 + ks) * 64 + lane) * 8);
            __hip_atomic_store(dst, (unsigned long long)ov[0] | ((unsigned long long)ov[1] << 32), __ATOMIC_RELAXED, __HIP_MEMORY_SCOPE_AGENT);
            __hip_atomic_store(dst + 1, (unsigned long long)ov[2] | ((unsigned long long)ov[3] << 32), __ATOMIC_RELAXED, __HIP_MEMORY_SCOPE_AGENT);
        }
        WAIT_V(0);
        __syncthreads();
        if (tid < 8) __hip_atomic_fetch_add(cnt + tid * 64, 1u, __ATOMIC_RELAXED, __HIP_MEMORY_SCOPE_AGENT);
    }

#pragma unroll
    for (int j = 0; j < 10; ++j) {
        int c = j * 512 + tid;
        int row = c / 80, cc = c % 80;
        *(u32x4*)(oimg + row * (OST * 2) + cc * 16) = pv[j];
    }
    if (tid < 64) *(u32x4*)(oimg + tid * (OST * 2) + 1280) = (u32x4){0u, 0u, 0u, 0u};

    if (tid == 0) {
        unsigned spins = 0;
        while (__hip_atomic_load(cnt + (b & 7) * 64, __ATOMIC_RELAXED, __HIP_MEMORY_SCOPE_AGENT) < 32u) {
            __builtin_amdgcn_s_sleep(1);
            if (++spins > (1u << 24)) break;
        }
        __builtin_amdgcn_fence(__ATOMIC_ACQUIRE, "wavefront");
    }
    WAIT_L(0);
    BAR();

    s16x8 a0[4], a1[4];
    {
        const int row = lane & 31;
#pragma unroll
        for (int hh = 0; hh < 4; ++hh) {
            int h = wv * 4 + hh;
            u32x4 v0 = (u32x4){0u, 0u, 0u, 0u}, v1 = (u32x4){0u, 0u, 0u, 0u};
            if (row <= 20) {
                const unsigned long long* s0 = (const unsigned long long*)(kvfrag + ((size_t)(h * 2 + 0) * 64 + lane) * 8);
                unsigned long long t0 = __hip_atomic_load(s0, __ATOMIC_RELAXED, __HIP_MEMORY_SCOPE_AGENT);
                unsigned long long t1 = __hip_atomic_load(s0 + 1, __ATOMIC_RELAXED, __HIP_MEMORY_SCOPE_AGENT);
                v0 = (u32x4){(unsigned)t0, (unsigned)(t0 >> 32), (unsigned)t1, (unsigned)(t1 >> 32)};
                if (lane < 32) {
                    const unsigned long long* s1 = (const unsigned long long*)(kvfrag + ((size_t)(h * 2 + 1) * 64 + lane) * 8);
                    unsigned long long t2 = __hip_atomic_load(s1, __ATOMIC_RELAXED, __HIP_MEMORY_SCOPE_AGENT);
                    unsigned long long t3 = __hip_atomic_load(s1 + 1, __ATOMIC_RELAXED, __HIP_MEMORY_SCOPE_AGENT);
                    v1 = (u32x4){(unsigned)t2, (unsigned)(t2 >> 32), (unsigned)t3, (unsigned)(t3 >> 32)};
                }
            }
            a0[hh] = __builtin_bit_cast(s16x8, v0);
            a1[hh] = __builtin_bit_cast(s16x8, v1);
        }
    }
    {
        const int hl = lane >> 5;
        char* rbase = oimg + (lane & 31) * (OST * 2) + wv * 160 + hl * 16;
        char* wbase = oimg + (lane & 31) * (OST * 2) + wv * 160 + hl * 8;
        f32x16 zero16;
#pragma unroll
        for (int i = 0; i < 16; ++i) zero16[i] = 0.f;
        u32x4 b0[8], b1[8];
#pragma unroll
        for (int u = 0; u < 8; ++u) {
            const char* rp = rbase + (u & 1) * 32 * (OST * 2) + (u >> 1) * 40;
            u32x2 l0 = *(const u32x2*)(rp), l1 = *(const u32x2*)(rp + 8);
            u32x2 l2 = *(const u32x2*)(rp + 32), l3 = *(const u32x2*)(rp + 40);
            b0[u] = (u32x4){l0[0], l0[1], l1[0], l1[1]};
            b1[u] = (u32x4){l2[0], l2[1], l3[0], l3[1]};
        }
#pragma unroll
        for (int half = 0; half < 2; ++half) {
            f32x16 dd[4];
#pragma unroll
            for (int v = 0; v < 4; ++v) {
                const int u = half * 4 + v;
                dd[v] = __builtin_amdgcn_mfma_f32_32x32x16_bf16(a0[u >> 1], __builtin_bit_cast(s16x8, b0[u]), zero16, 0, 0, 0);
            }
#pragma unroll
            for (int v = 0; v < 4; ++v) {
                const int u = half * 4 + v;
                dd[v] = __builtin_amdgcn_mfma_f32_32x32x16_bf16(a1[u >> 1], __builtin_bit_cast(s16x8, b1[u]), dd[v], 0, 0, 0);
            }
            __builtin_amdgcn_sched_barrier(0);
#pragma unroll
            for (int v = 0; v < 4; ++v) {
                const int u = half * 4 + v;
                unsigned du = __float_as_uint(dd[v][8]);
                auto sw = __builtin_amdgcn_permlane32_swap(du, du, false, false);
                float den = __uint_as_float(sw[1]);
                den = rbf(rbf(den) + 1e-6f);
                float inv = __builtin_amdgcn_rcpf(den);
                char* wp = wbase + (u & 1) * 32 * (OST * 2) + (u >> 1) * 40;
#pragma unroll
                for (int q = 0; q < 3; ++q) {
                    u32x2 vv;
                    unsigned n0 = pack2(dd[v][4 * q + 0], dd[v][4 * q + 1]), n1 = pack2(dd[v][4 * q + 2], dd[v][4 * q + 3]);
                    vv[0] = pack2(lo2f(n0) * inv, hi2f(n0) * inv);
                    vv[1] = pack2(lo2f(n1) * inv, hi2f(n1) * inv);
                    if (q < 2) *(u32x2*)(wp + q * 16) = vv;
                    else if (hl == 0) *(u32x2*)(wp + 32) = vv;
                }
            }
        }
    }

    const int wr = wv & 3, wc = wv >> 2;
    const f32x4 zero4 = (f32x4){0.f, 0.f, 0.f, 0.f};
    f32x4 acc[3];
#pragma unroll
    for (int n = 0; n < 3; ++n) acc[n] = zero4;
    int w_off[4];
#pragma unroll
    for (int ks = 0; ks < 4; ++ks) w_off[ks] = (wc * 48 + r16) * 256 + (((4 * ks + g) ^ r16) * 16);
    const int orow = wr * 16 + r16;
    const char* obase = oimg + orow * (OST * 2) + g * 16;

    auto phase_b = [&](auto NT) {
        constexpr int nt = decltype(NT)::value;
#pragma unroll 1
        for (int ch = 0; ch < 5; ++ch) {
            WAIT_L(0);
            BAR();
            const char* wb = wbuf + (ch >= 3 ? ch - 3 : ch) * K2_CH_BYTES;
            const char* ob = obase + ch * 256;
            s16x8 of[4], wf[4][nt];
#pragma unroll
            for (int ks = 0; ks < 4; ++ks) {
                of[ks] = *(const s16x8*)(ob + ks * 64);
#pragma unroll
                for (int n = 0; n < nt; ++n) {
                    wf[ks][n] = *(const s16x8*)(wb + w_off[ks] + n * 4096);
                }
            }
#pragma unroll
            for (int ks = 0; ks < 4; ++ks)
#pragma unroll
                for (int n = 0; n < nt; ++n)
                    acc[n] = __builtin_amdgcn_mfma_f32_16x16x32_bf16(wf[ks][n], of[ks], acc[n], 0, 0, 0);
        }
    };
    if (wc == 0) phase_b(std::integral_constant<int, 3>{});
    else         phase_b(std::integral_constant<int, 2>{});
    int s = row0 + orow;
#pragma unroll
    for (int n = 0; n < 3; ++n) {
        if (n == 2 && wc == 1) continue;
        u32x2 v;
        v[0] = pack2(acc[n][0], acc[n][1]);
        v[1] = pack2(acc[n][2], acc[n][3]);
        *(u32x2*)(out + (size_t)s * D_ + col0 + (wc * 3 + n) * 16 + 4 * g) = v;
    }
}

extern "C" void kernel_launch(void* const* d_in, const int* in_sizes, int n_in,
                              void* d_out, int out_size, void* d_ws, size_t ws_size,
                              hipStream_t stream) {
    const u16* x = (const u16*)d_in[0];
    const u16* wqkv = (const u16*)d_in[1];
    const u16* wo = (const u16*)d_in[2];
    u16* out = (u16*)d_out;
    char* ws = (char*)d_ws;
    u16* pq = (u16*)ws;
    float* part = (float*)(ws + 4u * 1024 * 1024);
    u16* kvfrag = (u16*)(ws + 7u * 1024 * 1024);
    unsigned* cnt = (unsigned*)(ws + 8u * 1024 * 1024);
    u16* woT = (u16*)(ws + 10u * 1024 * 1024);
    k1_kernel<<<232, 640, 0, stream>>>(x, wqkv, pq, part, cnt, wo, woT);
    k2_kernel<<<256, 640, 0, stream>>>(pq, part, kvfrag, cnt, woT, out);
}
```
